```python
import torch

def _softmax(x, axis=-1):
    return torch.softmax(x, dim=axis)
def _gelu(x):
    return torch.nn.functional.gelu(x, approximate="tanh")
def _silu(x):
    return torch.nn.functional.silu(x)
def _sigmoid(x):
    return torch.sigmoid(x)
def _layernorm(x, axis=-1, eps=1e-5):
    m = x.mean(dim=axis, keepdim=True)
    v = x.var(dim=axis, keepdim=True, unbiased=False)
    return (x - m) / torch.sqrt(v + eps)
def _scatter_add(tgt, idx, src, axis=0):
    return tgt.clone().index_add_(axis, idx, src)
def _segment_sum(src, seg, n):
    out = torch.zeros((n,) + src.shape[1:], dtype=src.dtype)
    return out.index_add_(0, seg, src)
def _pad(x, pw):
    flat = tuple(v for pair in reversed(pw) for v in pair)
    return torch.nn.functional.pad(x, flat)

kernel_name = 'concat_heads__mha_block_cached__mha_block_cached__near_ridge'

def setup_inputs():

    _w = (lambda a: (lambda t: t.to(torch.bfloat16) if t.is_floating_point() else t)(torch.as_tensor(a)))
    def _A_setup():
        B, Sq, D = 8, 144, 640
        H, Sk, Dh = 32, 512, 192
        return {
            "x": _w((lambda *s: torch.randn(*s, dtype=torch.bfloat16) * 0.8)(B, Sq, D)),
            "k_cache": _w((lambda *s: torch.randn(*s, dtype=torch.bfloat16) * 0.8)(B, H, Sk, Dh)),
            "v_cache": _w((lambda *s: torch.randn(*s, dtype=torch.bfloat16) * 0.05)(B, H, Sk, Dh)),
            "wq": _w((lambda *s: torch.randn(*s, dtype=torch.bfloat16) * 0.8)(D, H * Dh) * (D ** -0.5)),
            "wo": _w((lambda *s: torch.randn(*s, dtype=torch.bfloat16) * 0.05)(H * Dh, D) * ((H * Dh) ** -0.5)),
        }
    _A_in = _A_setup()

    def _B_setup():
        B, Sq, D = 8, 144, 640
        H, Sk, Dh = 32, 512, 192
        return {
            "x": _w((lambda *s: torch.randn(*s, dtype=torch.bfloat16) * 0.8)(B, Sq, D)),
            "k_cache": _w((lambda *s: torch.randn(*s, dtype=torch.bfloat16) * 0.8)(B, H, Sk, Dh)),
            "v_cache": _w((lambda *s: torch.randn(*s, dtype=torch.bfloat16) * 0.05)(B, H, Sk, Dh)),
            "wq": _w((lambda *s: torch.randn(*s, dtype=torch.bfloat16) * 0.8)(D, H * Dh) * (D ** -0.5)),
            "wo": _w((lambda *s: torch.randn(*s, dtype=torch.bfloat16) * 0.05)(H * Dh, D) * ((H * Dh) ** -0.5)),
        }
    _B_in = _B_setup()

    return {
        "A_x": _A_in["x"],
        "A_k_cache": _A_in["k_cache"],
        "A_v_cache": _A_in["v_cache"],
        "A_wq": _A_in["wq"],
        "A_wo": _A_in["wo"],
        "B_x": _B_in["x"],
        "B_k_cache": _B_in["k_cache"],
        "B_v_cache": _B_in["v_cache"],
        "B_wq": _B_in["wq"],
        "B_wo": _B_in["wo"],
    }

def reference(A_x, A_k_cache, A_v_cache, A_wq, A_wo, B_x, B_k_cache, B_v_cache, B_wq, B_wo):
    def _A_ref(x, k_cache, v_cache, wq, wo):
        B, Sq, D = x.shape
        H, Dh = 32, 192
        q = (lambda x, *d: x.permute(*d))((x @ wq).reshape(B, Sq, H, Dh), 0, 2, 1, 3)
        scores = torch.einsum("bhqd,bhkd->bhqk", q, k_cache) * (Dh ** -0.5)
        p = _softmax(scores, axis=-1)
        o = torch.einsum("bhqk,bhkd->bhqd", p, v_cache)
        return (lambda x, *d: x.permute(*d))(o, 0, 2, 1, 3).reshape(B, Sq, H * Dh) @ wo

    def _B_ref(x, k_cache, v_cache, wq, wo):
        B, Sq, D = x.shape
        H, Dh = 32, 192
        q = (lambda x, *d: x.permute(*d))((x @ wq).reshape(B, Sq, H, Dh), 0, 2, 1, 3)
        scores = torch.einsum("bhqd,bhkd->bhqk", q, k_cache) * (Dh ** -0.5)
        p = _softmax(scores, axis=-1)
        o = torch.einsum("bhqk,bhkd->bhqd", p, v_cache)
        return (lambda x, *d: x.permute(*d))(o, 0, 2, 1, 3).reshape(B, Sq, H * Dh) @ wo

    a = _A_ref(A_x, A_k_cache, A_v_cache, A_wq, A_wo)
    b = _B_ref(B_x, B_k_cache, B_v_cache, B_wq, B_wo)
    return torch.concatenate([a, b], axis=-1)
```

```cpp
#include <hip/hip_runtime.h>
#include <hip/hip_bf16.h>
#include <stdint.h>

typedef __attribute__((ext_vector_type(4))) short s16x4;
typedef __attribute__((ext_vector_type(8))) short s16x8;
typedef __attribute__((ext_vector_type(8))) __bf16 bf16x8;
typedef __attribute__((ext_vector_type(4))) __bf16 bf16x4;
typedef __attribute__((ext_vector_type(4))) float f32x4;
typedef __attribute__((ext_vector_type(2))) unsigned u32x2;
typedef __attribute__((ext_vector_type(4))) unsigned u32x4;
#define LDS3 __attribute__((address_space(3)))

constexpr int NB = 8, SQ = 144, DM = 640, NH = 32, SK = 512, DH = 192;
constexpr int UNIT = 24576;
constexpr int PBUF = 18432;
constexpr int L_PB = 5 * UNIT;
constexpr int L_M = L_PB + 2 * PBUF;
constexpr int L_L = L_M + 2 * SQ * 4;
constexpr int L_TOT1 = L_L + 4 * SQ * 4;
constexpr int L_Q = 4 * UNIT;
constexpr size_t OWS_ITEM = 55296;
constexpr size_t OWS_BYTES = 512 * OWS_ITEM;

#define WAIT_VM(n) asm volatile("s_waitcnt vmcnt(" #n ")" ::: "memory")
#define BAR() do { asm volatile("s_waitcnt lgkmcnt(0)" ::: "memory"); __builtin_amdgcn_s_barrier(); asm volatile("" ::: "memory"); } while (0)

__device__ __forceinline__ void dma16(unsigned lds, unsigned voff, const char* base) {
  asm volatile("s_mov_b32 m0, %0\n\ts_nop 0\n\tglobal_load_lds_dwordx4 %1, %2"
               :: "s"(lds), "v"(voff), "s"(base) : "memory");
}
__device__ __forceinline__ bf16x8 lds_b128(unsigned a) {
  return *(const LDS3 bf16x8*)(uintptr_t)a;
}
__device__ __forceinline__ bf16x8 lds_tr2(unsigned a0, unsigned a1) {
  s16x4 x = __builtin_amdgcn_ds_read_tr16_b64_v4i16((LDS3 s16x4*)(uintptr_t)a0);
  s16x4 y = __builtin_amdgcn_ds_read_tr16_b64_v4i16((LDS3 s16x4*)(uintptr_t)a1);
  s16x8 z = __builtin_shufflevector(x, y, 0, 1, 2, 3, 4, 5, 6, 7);
  return __builtin_bit_cast(bf16x8, z);
}
__device__ __forceinline__ u32x2 pack4(float a, float b, float c, float d) {
  bf16x4 p; p[0] = (__bf16)a; p[1] = (__bf16)b; p[2] = (__bf16)c; p[3] = (__bf16)d;
  return __builtin_bit_cast(u32x2, p);
}
__device__ __forceinline__ int opaque(int v) { asm volatile("" : "+v"(v)); return v; }
#define MFMA(a, b, c) __builtin_amdgcn_mfma_f32_16x16x32_bf16((a), (b), (c), 0, 0, 0)

struct K1Args {
  const char* x[2]; const char* k[2]; const char* v[2]; const char* wq[2];
  char* ows;
};

template <int NP>
__device__ __forceinline__ void pv_step(f32x4 (&acc)[NP][3], const unsigned (&va)[3], unsigned pa0, unsigned pa1) {
#pragma unroll
  for (int ks = 0; ks < 2; ++ks) {
    bf16x8 a[3];
#pragma unroll
    for (int i = 0; i < 3; ++i) a[i] = lds_tr2(va[i] + ks * 12288, va[i] + ks * 12288 + 1536);
#pragma unroll
    for (int t = 0; t < NP; ++t) {
      bf16x8 b = lds_b128((ks ? pa1 : pa0) + t * 2048);
#pragma unroll
      for (int i = 0; i < 3; ++i) acc[t][i] = MFMA(a[i], b, acc[t][i]);
    }
  }
}

template <int G>
__device__ __forceinline__ void k1_body(const K1Args& A, int cg, int lane_in, int b, int h) {
  const int lane = opaque(lane_in);
  constexpr int NQ = G == 0 ? 5 : 4, QT0 = G == 0 ? 0 : 5;
  constexpr int NP = G == 0 ? 4 : 5, PT0 = G == 0 ? 0 : 4;
  constexpr int NM = 5;
  constexpr int MT0 = G == 0 ? 0 : 4;
  constexpr int QO = QT0 - MT0;
  const float cs = 0.07216878364870322f * 1.4426950408889634f;
  const int li = lane & 15, g = lane >> 4;
  const unsigned fv2 = (unsigned)((((li >> 1) & 1) | ((li >> 2) & 2)) << 1);
  const unsigned fk = (unsigned)(li >> 1);
  const unsigned kofs0 = (unsigned)((16 * cg + li) * 384) + (((unsigned)g ^ fv2) << 4);
  const unsigned kofs1 = kofs0 ^ 64u;
  const unsigned pofs0 = L_PB + (unsigned)((16 * PT0 + li) * 128) + (((unsigned)g ^ fk) << 4);
  const unsigned pofs1 = pofs0 ^ 64u;
  unsigned vofs[3];
  {
    const int q = (lane >> 2) & 3, p = lane & 3;
    const unsigned fv = (unsigned)(((q >> 1) & 1) | ((g & 1) << 1));
#pragma unroll
    for (int i = 0; i < 3; ++i) {
      unsigned t = (unsigned)(3 * cg + i);
      vofs[i] = (unsigned)((8 * g + q) * 384) + ((t ^ fv) << 5) + (unsigned)(p * 8);
    }
  }
  const unsigned pw = L_PB + (unsigned)((16 * QT0 + li) * 128) + ((((unsigned)(2 * cg + (g >> 1))) ^ fk) << 4) + (unsigned)((g & 1) * 8);
  const unsigned mofs = L_M + (unsigned)((16 * MT0 + li) * 4);
  unsigned dofs[6];
  unsigned xofs = 0;
  auto mk_dofs = [&]() {
    const int ln = opaque(lane);
#pragma unroll
    for (int j = 0; j < 6; ++j) {
      int s = 64 * j + ln, r = (s * 2731) >> 16, c = s - r * 24;
      int f = (((r >> 1) & 1) | ((r >> 2) & 2)) << 1;
      int ch = c ^ f;
      dofs[j] = G == 0 ? (unsigned)((16 * cg + r) * (NH * DH * 2) + ch * 16) : (unsigned)((16 * cg + r) * 384 + ch * 16);
    }
    if (G == 0) xofs = (unsigned)((ln >> 3) * (DM * 2) + (((ln & 7) ^ ((4 * (cg & 1) + (ln >> 4)) & 7)) << 4));
  };
  mk_dofs();

  const size_t kvoff = (size_t)(b * NH + h) * SK * DH * 2;
  auto issue_unit = [&](const char* src, int unit) {
#pragma unroll
    for (int j = 0; j < 6; ++j) dma16((unsigned)(unit * UNIT + cg * 6144 + j * 1024), dofs[j], src);
  };
  auto issue_x = [&](const char* src, int pb) {
#pragma unroll
    for (int i = 0; i < 5; ++i) {
      int p = cg + 4 * i; if (p > 17) p -= 2;
      dma16((unsigned)(L_PB + pb * PBUF + p * 1024), xofs, src + (size_t)p * 8 * DM * 2);
    }
  };

  if (G == 0) {
    const char* xb = A.x[0] + (size_t)b * SQ * DM * 2;
    const char* wb = A.wq[0] + (size_t)h * DH * 2;
    issue_unit(wb, 2);
    issue_x(xb, 0);
    issue_unit(wb + (size_t)64 * NH * DH * 2, 3);
    issue_unit(wb + (size_t)128 * NH * DH * 2, 4);
    WAIT_VM(12);
  } else {
    issue_unit(A.k[0] + kvoff + UNIT, 1);
  }
  BAR();

  for (int it = 0; it < 2; ++it) {
    const char* xb = A.x[it] + (size_t)b * SQ * DM * 2;
    const char* wb = A.wq[it] + (size_t)h * DH * 2;
    const char* kb = A.k[it] + kvoff;
    const char* vb = A.v[it] + kvoff;
    f32x4 acc[NP][3];
#pragma unroll
    for (int t = 0; t < NP; ++t)
#pragma unroll
      for (int i = 0; i < 3; ++i) acc[t][i] = (f32x4){0.f, 0.f, 0.f, 0.f};
    {
      int tid = G * 256 + cg * 64 + opaque(lane);
      if (tid < 2 * SQ) *(LDS3 float*)(uintptr_t)(L_M + tid * 4) = -__builtin_inff();
    }
    int wu = 2;
    for (int j = 0; j < 10; ++j) {
      if (G == 0) {
        if (j < 9) issue_x(xb + (j + 1) * 128, (j + 1) & 1);
        if (j >= 1 && j <= 7) issue_unit(wb + (size_t)(j + 2) * 64 * NH * DH * 2, wu == 2 ? 4 : wu - 1);
      } else {
        if (j == 0) issue_unit(kb, 0);
        if (j == 8) issue_unit(kb + 2 * UNIT, 3);
      }
      unsigned va[3];
#pragma unroll
      for (int i = 0; i < 3; ++i) va[i] = vofs[i] + (unsigned)(wu * UNIT);
      pv_step<NP>(acc, va, pofs0 + (unsigned)((j & 1) * PBUF), pofs1 + (unsigned)((j & 1) * PBUF));
      if (G == 0) { if (j >= 1 && j <= 7) WAIT_VM(6); else WAIT_VM(0); }
      BAR();
      wu = wu == 4 ? 2 : wu + 1;
    }
    if (G == 1) issue_unit(vb, 2);
    const int lq = opaque(lane), liq = lq & 15, gq = lq >> 4;
    const unsigned fkq = (unsigned)(liq >> 1);
#pragma unroll
    for (int t = 0; t < NP; ++t)
#pragma unroll
      for (int i = 0; i < 3; ++i) {
        unsigned ch = (unsigned)(2 * (3 * cg + i) + (gq >> 1));
        unsigned a = L_Q + (unsigned)((16 * (PT0 + t) + liq) * 384) + ((ch ^ fkq) << 4) + (unsigned)((gq & 1) * 8);
        *(LDS3 u32x2*)(uintptr_t)a = pack4(acc[t][i][0], acc[t][i][1], acc[t][i][2], acc[t][i][3]);
        acc[t][i] = (f32x4){0.f, 0.f, 0.f, 0.f};
      }
    BAR();
    bf16x8 qf[NQ][6];
    {
      unsigned q0 = L_Q + (unsigned)((16 * QT0 + liq) * 384) + (((unsigned)gq ^ fkq) << 4);
#pragma unroll
      for (int t = 0; t < NQ; ++t)
#pragma unroll
        for (int ks = 0; ks < 6; ++ks) qf[t][ks] = lds_b128(((ks & 1) ? (q0 ^ 64u) : q0) + t * 6144 + (ks >> 1) * 128);
    }
    if (G == 1) WAIT_VM(12);
    BAR();
    if (G == 1) issue_unit(vb + UNIT, 4);

    f32x4 s[NQ];
    float mcur[NM], lsum[NQ];
#pragma unroll
    for (int t = 0; t < NM; ++t) mcur[t] = -__builtin_inff();
#pragma unroll
    for (int t = 0; t < NQ; ++t) lsum[t] = 0.f;

    auto qk = [&](int unit, int par) {
      unsigned k0 = kofs0 + (unsigned)(unit * UNIT), k1 = kofs1 + (unsigned)(unit * UNIT);
#pragma unroll
      for (int t = 0; t < NQ; ++t) s[t] = (f32x4){0.f, 0.f, 0.f, 0.f};
#pragma unroll
      for (int ks = 0; ks < 6; ++ks) {
        bf16x8 a = lds_b128(((ks & 1) ? k1 : k0) + (ks >> 1) * 128);
#pragma unroll
        for (int t = 0; t < NQ; ++t) s[t] = MFMA(a, qf[t][ks], s[t]);
      }
#pragma unroll
      for (int t = 0; t < NQ; ++t) {
        float mx = fmaxf(fmaxf(s[t][0], s[t][1]), fmaxf(fmaxf(s[t][2], s[t][3]), mcur[QO + t]));
        __hip_atomic_fetch_max((LDS3 float*)(uintptr_t)(mofs + (unsigned)(par * SQ * 4 + (QO + t) * 64)), mx,
                               __ATOMIC_RELAXED, __HIP_MEMORY_SCOPE_WORKGROUP);
      }
    };
    float alpha[NM];
    auto softmax = [&](int par) {
#pragma unroll
      for (int t = 0; t < NM; ++t) {
        float mn = *(const LDS3 float*)(uintptr_t)(mofs + (unsigned)(par * SQ * 4 + t * 64));
        alpha[t] = __builtin_amdgcn_exp2f((mcur[t] - mn) * cs);
        mcur[t] = mn;
      }
#pragma unroll
      for (int t = 0; t < NQ; ++t) {
        float mc = -mcur[QO + t] * cs;
        float p0 = __builtin_amdgcn_exp2f(fmaf(s[t][0], cs, mc));
        float p1 = __builtin_amdgcn_exp2f(fmaf(s[t][1], cs, mc));
        float p2 = __builtin_amdgcn_exp2f(fmaf(s[t][2], cs, mc));
        float p3 = __builtin_amdgcn_exp2f(fmaf(s[t][3], cs, mc));
        lsum[t] = fmaf(lsum[t], alpha[QO + t], (p0 + p1) + (p2 + p3));
        *(LDS3 u32x2*)(uintptr_t)(pw + (unsigned)(par * PBUF + t * 2048)) = pack4(p0, p1, p2, p3);
      }
    };
    auto rescale = [&]() {
#pragma unroll
      for (int t = 0; t < NP; ++t)
#pragma unroll
        for (int i = 0; i < 3; ++i) acc[t][i] *= alpha[(PT0 - MT0) + t];
    };
    auto pv = [&](int unit, int par) {
      unsigned va[3];
#pragma unroll
      for (int i = 0; i < 3; ++i) va[i] = vofs[i] + (unsigned)(unit * UNIT);
      pv_step<NP>(acc, va, pofs0 + (unsigned)(par * PBUF), pofs1 + (unsigned)(par * PBUF));
    };

    qk(0, 0);
    BAR();
    if (G == 1) issue_unit(kb + 3 * UNIT, 0);
    softmax(0);
    qk(1, 1);
    if (G == 1) WAIT_VM(12);
    BAR();
    int uv = 2;
    for (int c = 1; c <= 6; ++c) {
      int uk = uv == 4 ? 0 : uv + 1;
      int u3 = uv >= 2 ? uv - 2 : uv + 3;
      int u4 = uv >= 1 ? uv - 1 : uv + 4;
      if (G == 1) {
        if (c <= 5) {
          if (c >= 2) issue_unit(kb + (size_t)(c + 2) * UNIT, u3);
          issue_unit(vb + (size_t)(c + 1) * UNIT, u4);
        } else {
          issue_unit(vb + (size_t)7 * UNIT, u3);
          if (it == 0) issue_unit(A.k[1] + kvoff + UNIT, u4);
        }
      }
      softmax(c & 1);
      pv(uv, (c - 1) & 1);
      rescale();
      qk(uk, (c + 1) & 1);
      if (G == 1) { if (c == 6 && it == 0) WAIT_VM(12); else WAIT_VM(6); }
      BAR();
      uv = uv >= 3 ? uv - 3 : uv + 2;
    }
    if (G == 0) mk_dofs();
    if (G == 0 && it == 0) {
      const char* wn = A.wq[1] + (size_t)h * DH * 2;
      issue_unit(wn, 2);
      issue_unit(wn + (size_t)64 * NH * DH * 2, 3);
    }
    softmax(1);
    pv(4, 0);
    rescale();
    const int le = opaque(lane);
#pragma unroll
    for (int t = 0; t < NQ; ++t) {
      float l = lsum[t];
      l += __shfl_xor(l, 16);
      l += __shfl_xor(l, 32);
      if (le < 16) *(LDS3 float*)(uintptr_t)(L_L + (unsigned)(cg * SQ * 4 + (16 * (QT0 + t) + le) * 4)) = l;
    }
    if (G == 1) { if (it == 0) WAIT_VM(6); else WAIT_VM(0); }
    BAR();
    if (G == 0 && it == 0) {
      issue_x(A.x[1] + (size_t)b * SQ * DM * 2, 0);
      issue_unit(A.wq[1] + (size_t)h * DH * 2 + (size_t)128 * NH * DH * 2, 4);
    }
    pv(0, 1);
    {
      char* ob = A.ows + (size_t)((it * NB + b) * NH + h) * OWS_ITEM + le * 16;
      const int uf = (3 * cg + 1) >> 1;
      const int ufull = (cg & 1) ? uf : uf;
      const int uhalf = (cg & 1) ? (3 * cg) >> 1 : (3 * cg + 2) >> 1;
#pragma unroll
      for (int t = 0; t < NP; ++t) {
        unsigned la = L_L + (unsigned)((16 * (PT0 + t) + (le & 15)) * 4);
        float l = (*(const LDS3 float*)(uintptr_t)la + *(const LDS3 float*)(uintptr_t)(la + SQ * 4)) +
                  (*(const LDS3 float*)(uintptr_t)(la + 2 * SQ * 4) + *(const LDS3 float*)(uintptr_t)(la + 3 * SQ * 4));
        float inv = 1.0f / l;
        u32x2 o[3];
#pragma unroll
        for (int i = 0; i < 3; ++i) o[i] = pack4(acc[t][i][0] * inv, acc[t][i][1] * inv, acc[t][i][2] * inv, acc[t][i][3] * inv);
        u32x4 full; u32x2 half;
        if (cg & 1) { full = (u32x4){o[1][0], o[1][1], o[2][0], o[2][1]}; half = o[0]; }
        else        { full = (u32x4){o[0][0], o[0][1], o[1][0], o[1][1]}; half = o[2]; }
        *(u32x4*)(ob + (ufull >> 1) * PBUF + (PT0 + t) * 2048 + (ufull & 1) * 1024) = full;
        *(u32x2*)(ob + (uhalf >> 1) * PBUF + (PT0 + t) * 2048 + (uhalf & 1) * 1024 + (cg & 1) * 8) = half;
      }
    }
    if (G == 0 && it == 0) WAIT_VM(14);
    BAR();
  }
}

__global__ __launch_bounds__(512, 2) void k1_attn(K1Args A) {
  extern __shared__ __attribute__((aligned(16))) char shm[];
  const int w = __builtin_amdgcn_readfirstlane(threadIdx.x >> 6);
  const int lane = threadIdx.x & 63;
  const int xcd = blockIdx.x & 7, idx = blockIdx.x >> 3;
  const int h = xcd * 4 + (idx & 3), b = idx >> 2;
  if (w < 4) k1_body<0>(A, w, lane, b, h);
  else k1_body<1>(A, w - 4, lane, b, h);
}

constexpr int K2_STAGE = PBUF + 64 * 320;
constexpr int K2_NST = 4;
constexpr int K2_CH = 24;
struct K2Args { const char* wo[2]; const char* ows; float* part; };

template <int NR, int NC>
__device__ __forceinline__ void k2_body(const K2Args& A, int w, int lane, int rt0, int ct0, int br, int b, int nb, int sp) {
  const int g = lane >> 4;
  unsigned dofs[5]; int dlds[5]; bool isw[5];
#pragma unroll
  for (int i = 0; i < 5; ++i) {
    int p = w + 8 * i; if (p >= 38) p -= 2;
    isw[i] = p >= 18;
    dlds[i] = p * 1024;
    if (p < 18) dofs[i] = (unsigned)(p * 1024 + lane * 16);
    else {
      int s = 64 * (p - 18) + lane, r = s / 20, c = s - r * 20;
      dofs[i] = (unsigned)(r * (DM * 2) + ((c ^ (((r >> 2) & 1) << 1)) << 4));
    }
  }
  const char* ob = A.ows + ((size_t)(br * NB + b) * NH * 3 + (size_t)sp * K2_CH) * PBUF;
  const char* wb = A.wo[br] + (size_t)sp * K2_CH * 64 * DM * 2 + nb * 320;
  auto issue = [&](int ci) {
    unsigned base = (unsigned)((ci & 3) * K2_STAGE);
#pragma unroll
    for (int i = 0; i < 5; ++i)
      dma16(base + (unsigned)dlds[i], dofs[i], isw[i] ? wb + (size_t)ci * 64 * DM * 2 : ob + (size_t)ci * PBUF);
  };
  unsigned aofs[NC];
  {
    const int q = (lane >> 2) & 3, p = lane & 3;
#pragma unroll
    for (int i = 0; i < NC; ++i) aofs[i] = PBUF + (unsigned)((4 * g + q) * 320 + (((ct0 + i) ^ (g & 1)) << 5) + p * 8);
  }
  const unsigned bofs = (unsigned)(rt0 * 2048 + lane * 16);
  f32x4 acc[NR][NC];
#pragma unroll
  for (int t = 0; t < NR; ++t)
#pragma unroll
    for (int i = 0; i < NC; ++i) acc[t][i] = (f32x4){0.f, 0.f, 0.f, 0.f};
  issue(0); issue(1); issue(2);
  for (int ci = 0; ci < K2_CH; ++ci) {
    if (ci < K2_CH - 2) WAIT_VM(10); else if (ci == K2_CH - 2) WAIT_VM(5); else WAIT_VM(0);
    BAR();
    if (ci + 3 < K2_CH) issue(ci + 3);
    unsigned base = (unsigned)((ci & 3) * K2_STAGE);
#pragma unroll
    for (int ks = 0; ks < 2; ++ks) {
      bf16x8 a[NC];
#pragma unroll
      for (int i = 0; i < NC; ++i) a[i] = lds_tr2(base + aofs[i] + ks * 10240, base + aofs[i] + ks * 10240 + 5120);
#pragma unroll
      for (int t = 0; t < NR; ++t) {
        bf16x8 bb = lds_b128(base + bofs + t * 2048 + ks * 1024);
#pragma unroll
        for (int i = 0; i < NC; ++i) acc[t][i] = MFMA(a[i], bb, acc[t][i]);
      }
    }
  }
  float* pb = A.part + ((size_t)(((br * NB + b) * 4 + nb) * 4 + sp) * 90) * 256 + lane * 4;
#pragma unroll
  for (int t = 0; t < NR; ++t)
#pragma unroll
    for (int i = 0; i < NC; ++i) *(f32x4*)(pb + ((rt0 + t) * 10 + ct0 + i) * 256) = acc[t][i];
}

__global__ __launch_bounds__(512, 2) void k2_oproj(K2Args A) {
  extern __shared__ __attribute__((aligned(16))) char shm[];
  const int w = __builtin_amdgcn_readfirstlane(threadIdx.x >> 6);
  const int lane = threadIdx.x & 63;
  const int xcd = blockIdx.x & 7, idx = blockIdx.x >> 3;
  const int br = xcd >> 2, sp = xcd & 3, b = idx >> 2, nb = idx & 3;
  const int cg = w & 3;
  if (w < 2) k2_body<5, 3>(A, w, lane, 0, 3 * cg, br, b, nb, sp);
  else if (w < 4) k2_body<5, 2>(A, w, lane, 0, 2 * cg + 2, br, b, nb, sp);
  else if (w < 6) k2_body<4, 2>(A, w, lane, 5, 2 * cg, br, b, nb, sp);
  else k2_body<4, 3>(A, w, lane, 5, 3 * cg - 2, br, b, nb, sp);
}

__global__ __launch_bounds__(256) void k3_reduce(const float* __restrict__ part, char* __restrict__ out) {
  int gid = blockIdx.x * 256 + threadIdx.x;
  int lane = gid & 63, f = (gid >> 6) % 90, tile = (gid >> 6) / 90;
  const float* p = part + ((size_t)tile * 4 * 90 + f) * 256 + lane * 4;
  f32x4 s = *(const f32x4*)p;
  s += *(const f32x4*)(p + 90 * 256);
  s += *(const f32x4*)(p + 2 * 90 * 256);
  s += *(const f32x4*)(p + 3 * 90 * 256);
  int nb = tile & 3, b = (tile >> 2) & 7, br = tile >> 5;
  int rt = f / 10, ct = f - rt * 10;
  int q = rt * 16 + (lane & 15), n = br * DM + nb * 160 + ct * 16 + (lane >> 4) * 4;
  *(u32x2*)(out + ((size_t)(b * SQ + q) * (2 * DM) + n) * 2) = pack4(s[0], s[1], s[2], s[3]);
}

extern "C" void kernel_launch(void* const* d_in, const int* in_sizes, int n_in, void* d_out, int out_size,
                              void* d_ws, size_t ws_size, hipStream_t stream) {
  (void)in_sizes; (void)n_in; (void)out_size; (void)ws_size;
  K1Args a1;
  for (int i = 0; i < 2; ++i) {
    a1.x[i] = (const char*)d_in[5 * i + 0];
    a1.k[i] = (const char*)d_in[5 * i + 1];
    a1.v[i] = (const char*)d_in[5 * i + 2];
    a1.wq[i] = (const char*)d_in[5 * i + 3];
  }
  a1.ows = (char*)d_ws;
  K2Args a2;
  a2.wo[0] = (const char*)d_in[4];
  a2.wo[1] = (const char*)d_in[9];
  a2.ows = (const char*)d_ws;
  a2.part = (float*)((char*)d_ws + OWS_BYTES);
  (void)hipFuncSetAttribute((const void*)k1_attn, hipFuncAttributeMaxDynamicSharedMemorySize, L_TOT1);
  (void)hipFuncSetAttribute((const void*)k2_oproj, hipFuncAttributeMaxDynamicSharedMemorySize, K2_NST * K2_STAGE);
  k1_attn<<<256, 512, L_TOT1, stream>>>(a1);
  k2_oproj<<<256, 512, K2_NST * K2_STAGE, stream>>>(a2);
  k3_reduce<<<64 * 90 * 64 / 256, 256, 0, stream>>>(a2.part, (char*)d_out);
}
```

```cpp
#include <hip/hip_runtime.h>
#include <hip/hip_bf16.h>
#include <stdint.h>

typedef __attribute__((ext_vector_type(4))) short s16x4;
typedef __attribute__((ext_vector_type(8))) short s16x8;
typedef __attribute__((ext_vector_type(8))) __bf16 bf16x8;
typedef __attribute__((ext_vector_type(4))) __bf16 bf16x4;
typedef __attribute__((ext_vector_type(4))) float f32x4;
typedef __attribute__((ext_vector_type(2))) float f32x2;
typedef __attribute__((ext_vector_type(2))) unsigned u32x2;
typedef __attribute__((ext_vector_type(4))) unsigned u32x4;
#define LDS3 __attribute__((address_space(3)))

constexpr int NB = 8, SQ = 144, DM = 640, NH = 32, SK = 512, DH = 192;
constexpr int UNIT = 24576;
constexpr int PBUF = 18432;
constexpr int L_PB = 5 * UNIT;
constexpr int L_M = L_PB + 2 * PBUF;
constexpr int L_L = L_M + 2 * SQ * 4;
constexpr int L_TOT1 = L_L + 4 * SQ * 4;
constexpr int L_Q = 4 * UNIT;
constexpr size_t OWS_ITEM = 55296;
constexpr size_t OWS_BYTES = 512 * OWS_ITEM;

#define WAIT_VM(n) asm volatile("s_waitcnt vmcnt(" #n ")" ::: "memory")
#define STAMP_INIT() do {} while (0)
#define STAMP() do {} while (0)
#define BAR() do { asm volatile("s_waitcnt lgkmcnt(0)" ::: "memory"); __builtin_amdgcn_s_barrier(); asm volatile("" ::: "memory"); STAMP(); } while (0)

__device__ __forceinline__ void dma16(unsigned lds, unsigned voff, const char* base) {
  asm volatile("s_mov_b32 m0, %0\n\ts_nop 0\n\tglobal_load_lds_dwordx4 %1, %2"
               :: "s"(lds), "v"(voff), "s"(base) : "memory");
}
__device__ __forceinline__ bf16x8 lds_b128(unsigned a) {
  return *(const LDS3 bf16x8*)(uintptr_t)a;
}
__device__ __forceinline__ bf16x8 lds_tr2(unsigned a0, unsigned a1) {
  s16x4 x = __builtin_amdgcn_ds_read_tr16_b64_v4i16((LDS3 s16x4*)(uintptr_t)a0);
  s16x4 y = __builtin_amdgcn_ds_read_tr16_b64_v4i16((LDS3 s16x4*)(uintptr_t)a1);
  s16x8 z = __builtin_shufflevector(x, y, 0, 1, 2, 3, 4, 5, 6, 7);
  return __builtin_bit_cast(bf16x8, z);
}
__device__ __forceinline__ u32x2 pack4(float a, float b, float c, float d) {
  bf16x4 p; p[0] = (__bf16)a; p[1] = (__bf16)b; p[2] = (__bf16)c; p[3] = (__bf16)d;
  return __builtin_bit_cast(u32x2, p);
}
__device__ __forceinline__ int lane_id() {
  int r; asm volatile("v_mbcnt_lo_u32_b32 %0, -1, 0\n\tv_mbcnt_hi_u32_b32 %0, -1, %0" : "=v"(r)); return r;
}
__device__ __forceinline__ unsigned addv(unsigned v, int sc) {
  unsigned r; asm volatile("v_add_u32 %0, %1, %2" : "=v"(r) : "s"(sc), "v"(v)); return r;
}
__device__ __forceinline__ f32x4 zero_acc() {
  float z; asm volatile("v_mov_b32 %0, 0" : "=v"(z)); return (f32x4){z, z, z, z};
}
#define PIN(x) asm volatile("" : "+v"(x))
#define MFMA(a, b, c) __builtin_amdgcn_mfma_f32_16x16x32_bf16((a), (b), (c), 0, 0, 0)

struct K1Args {
  const char* x[2]; const char* k[2]; const char* v[2]; const char* wq[2];
  char* ows;
};

#define SB() __builtin_amdgcn_sched_barrier(0)
__device__ __forceinline__ void dma16x3(unsigned lds, unsigned v0, unsigned v1, unsigned v2, const char* base) {
  asm volatile("s_mov_b32 m0, %0\n\ts_nop 0\n\tglobal_load_lds_dwordx4 %1, %4\n\t"
               "global_load_lds_dwordx4 %2, %4 offset:1024\n\tglobal_load_lds_dwordx4 %3, %4 offset:2048"
               :: "s"(lds), "v"(v0), "v"(v1), "v"(v2), "s"(base) : "memory");
}

template <int G>
__device__ __forceinline__ void k1_body(const K1Args& A, int cg, int b, int h) {
  const int lane = lane_id();
  STAMP_INIT();
  constexpr int NQ = G == 0 ? 5 : 4, QT0 = G == 0 ? 0 : 5;
  constexpr int NP = G == 0 ? 4 : 5, PT0 = G == 0 ? 0 : 4;
  constexpr int NM = 5;
  constexpr int MT0 = G == 0 ? 0 : 4;
  constexpr int QO = QT0 - MT0;
  const float cs = 0.07216878364870322f * 1.4426950408889634f;
  const int li = lane & 15, g = lane >> 4;
  const unsigned fv2 = (unsigned)((((li >> 1) & 1) | ((li >> 2) & 2)) << 1);
  const unsigned fk = (unsigned)(li >> 1);
  const unsigned kofs0 = (unsigned)((16 * cg + li) * 384) + (((unsigned)g ^ fv2) << 4);
  const unsigned pofs0 = L_PB + (unsigned)((16 * PT0 + li) * 128) + (((unsigned)g ^ fk) << 4);
  unsigned vofs[3];
  {
    const int q = (lane >> 2) & 3, p = lane & 3;
    const unsigned fv = (unsigned)(((q >> 1) & 1) | ((g & 1) << 1));
#pragma unroll
    for (int i = 0; i < 3; ++i) {
      unsigned t = (unsigned)(3 * cg + i);
      vofs[i] = (unsigned)((8 * g + q) * 384) + ((t ^ fv) << 5) + (unsigned)(p * 8);
    }
  }
  const unsigned pw = L_PB + (unsigned)((16 * QT0 + li) * 128) + ((((unsigned)(2 * cg + (g >> 1))) ^ fk) << 4) + (unsigned)((g & 1) * 8);
  const unsigned mofs = L_M + (unsigned)((16 * MT0 + li) * 4);
  unsigned dofs[6];
  unsigned kvo[3];
  unsigned xofs = 0;
  auto mk_dofs = [&]() {
    const int ln = lane_id();
#pragma unroll
    for (int j = 0; j < 6; ++j) {
      int s = 64 * j + ln, r = (s * 2731) >> 16, c = s - r * 24;
      int f = (((r >> 1) & 1) | ((r >> 2) & 2)) << 1;
      int ch = c ^ f;
      dofs[j] = (unsigned)((16 * cg + r) * (G == 0 ? NH * DH * 2 : 384) + ch * 16 - (j % 3) * 1024);
    }
    if (G == 0) xofs = (unsigned)((ln >> 3) * (DM * 2) + (((ln & 7) ^ ((4 * (cg & 1) + (ln >> 4)) & 7)) << 4));
  };
  auto mk_kvo = [&]() {
    const int ln = lane_id();
#pragma unroll
    for (int j = 0; j < 3; ++j) {
      int s = 64 * j + ln, r = (s * 2731) >> 16, c = s - r * 24;
      kvo[j] = (unsigned)((16 * cg + r) * 384 + (c ^ (((r >> 1) & 1) << 1)) * 16 - j * 1024);
    }
  };
  mk_dofs();

  const size_t kvoff = (size_t)(b * NH + h) * SK * DH * 2;
  auto issue_unit = [&](const char* src, int unit) {
    dma16x3((unsigned)(unit * UNIT + cg * 6144), dofs[0], dofs[1], dofs[2], src);
    dma16x3((unsigned)(unit * UNIT + cg * 6144 + 3072), dofs[3], dofs[4], dofs[5], src);
  };
  auto issue_kv = [&](const char* src, int unit) {
    if (G == 0) dma16x3((unsigned)(unit * UNIT + cg * 6144), kvo[0], kvo[1], kvo[2], src);
    else dma16x3((unsigned)(unit * UNIT + cg * 6144 + 3072), dofs[3], dofs[4], dofs[5], src);
  };
  auto issue_x = [&](const char* src, int pb) {
#pragma unroll
    for (int i = 0; i < 5; ++i) {
      int p = cg + 4 * i; if (p > 17) p -= 2;
      dma16((unsigned)(L_PB + pb * PBUF + p * 1024), xofs, src + (size_t)p * 8 * DM * 2);
    }
  };

  if (G == 0) {
    const char* xb = A.x[0] + (size_t)b * SQ * DM * 2;
    const char* wb = A.wq[0] + (size_t)h * DH * 2;
    issue_unit(wb, 2);
    issue_x(xb, 0);
    issue_unit(wb + (size_t)64 * NH * DH * 2, 3);
    issue_unit(wb + (size_t)128 * NH * DH * 2, 4);
    WAIT_VM(12);
  } else {
    issue_unit(A.k[0] + kvoff + UNIT, 1);
  }
  BAR();

  f32x4 acc[NP][3];
  bf16x8 fa[2][3], fb[2][NP], fr[4];
  auto ld_set = [&](int ks, int unit, int par) {
#pragma unroll
    for (int i = 0; i < 3; ++i) {
      unsigned va = addv(vofs[i], unit * UNIT) + ks * 12288;
      fa[ks][i] = lds_tr2(va, va + 1536);
    }
    unsigned pa = addv(pofs0, par * PBUF) ^ (ks ? 64u : 0u);
#pragma unroll
    for (int t = 0; t < NP; ++t) fb[ks][t] = lds_b128(pa + t * 2048);
  };
  auto mm_set = [&](int ks) {
#pragma unroll
    for (int t = 0; t < NP; ++t)
#pragma unroll
      for (int i = 0; i < 3; ++i) acc[t][i] = MFMA(fa[ks][i], fb[ks][t], acc[t][i]);
  };

  for (int it = 0; it < 2; ++it) {
    const char* xb = A.x[it] + (size_t)b * SQ * DM * 2;
    const char* wb = A.wq[it] + (size_t)h * DH * 2;
    const char* kb = A.k[it] + kvoff;
    const char* vb = A.v[it] + kvoff;
#pragma unroll
    for (int t = 0; t < NP; ++t)
#pragma unroll
      for (int i = 0; i < 3; ++i) acc[t][i] = zero_acc();
    {
      int tid = G * 256 + cg * 64 + lane_id();
      if (tid < 2 * SQ) *(LDS3 float*)(uintptr_t)(L_M + tid * 4) = -__builtin_inff();
    }
    int wu = 2;
#pragma unroll
    for (int i = 0; i < 3; ++i) fa[1][i] = __builtin_bit_cast(bf16x8, zero_acc());
#pragma unroll
    for (int t = 0; t < NP; ++t) fb[1][t] = __builtin_bit_cast(bf16x8, zero_acc());
    for (int j = 0; j < 10; ++j) {
      ld_set(0, wu, j & 1);
      SB();
      if (G == 0) {
        if (j < 9) issue_x(xb + (j + 1) * 128, (j + 1) & 1);
        if (j >= 1 && j <= 7) issue_unit(wb + (size_t)(j + 2) * 64 * NH * DH * 2, wu == 2 ? 4 : wu - 1);
      } else {
        if (j == 0) { mk_dofs(); issue_unit(kb, 0); }
        if (j == 8) issue_unit(kb + 2 * UNIT, 3);
      }
      SB();
      if (j > 0) mm_set(1);
      SB();
      ld_set(1, wu, j & 1);
      SB();
      mm_set(0);
      SB();
      if (G == 0) { if (j >= 1 && j <= 7) WAIT_VM(6); else WAIT_VM(0); }
      BAR();
      wu = wu == 4 ? 2 : wu + 1;
    }
    if (G == 0) mk_kvo();
    issue_kv(vb, 2);
    mm_set(1);
    const int lq = lane_id(), liq = lq & 15, gq = lq >> 4;
    const unsigned fkq = (unsigned)(liq >> 1);
#pragma unroll
    for (int t = 0; t < NP; ++t)
#pragma unroll
      for (int i = 0; i < 3; ++i) {
        unsigned ch = (unsigned)(2 * (3 * cg + i) + (gq >> 1));
        unsigned a = L_Q + (unsigned)((16 * (PT0 + t) + liq) * 384) + ((ch ^ fkq) << 4) + (unsigned)((gq & 1) * 8);
        *(LDS3 u32x2*)(uintptr_t)a = pack4(acc[t][i][0], acc[t][i][1], acc[t][i][2], acc[t][i][3]);
        acc[t][i] = zero_acc();
      }
    BAR();
    bf16x8 qf[NQ][6];
    {
      unsigned q0 = L_Q + (unsigned)((16 * QT0 + liq) * 384) + (((unsigned)gq ^ fkq) << 4);
#pragma unroll
      for (int t = 0; t < NQ; ++t)
#pragma unroll
        for (int ks = 0; ks < 6; ++ks) qf[t][ks] = lds_b128(((ks & 1) ? (q0 ^ 64u) : q0) + t * 6144 + (ks >> 1) * 128);
    }
    if (G == 1) WAIT_VM(9);
    BAR();
    issue_kv(vb + UNIT, 4);

    f32x4 s[NQ];
    const f32x4 zero4 = {0.f, 0.f, 0.f, 0.f};
    float mcur[NM], lsum[NQ], alpha[NM];
    bf16x8 kf[3];
#pragma unroll
    for (int t = 0; t < NM; ++t) mcur[t] = -__builtin_inff();
#pragma unroll
    for (int t = 0; t < NQ; ++t) lsum[t] = 0.f;

    auto qk_pre = [&](int unit) {
      unsigned k0 = addv(kofs0, unit * UNIT);
      kf[0] = lds_b128(k0);
      kf[1] = lds_b128(k0 ^ 64u);
    };
    auto qk = [&](int unit, int par) {
      unsigned k0 = addv(kofs0, unit * UNIT), k1 = k0 ^ 64u;
#pragma unroll
      for (int ks = 0; ks < 6; ++ks) {
        if (ks + 2 < 6) kf[(ks + 2) % 3] = lds_b128(((ks & 1) ? k1 : k0) + ((ks + 2) >> 1) * 128);
        SB();
#pragma unroll
        for (int t = 0; t < NQ; ++t) s[t] = MFMA(kf[ks % 3], qf[t][ks], ks == 0 ? zero4 : s[t]);
        SB();
      }
    };
    auto pub_max = [&](int par) {
      const unsigned mb = addv(mofs, par * SQ * 4);
#pragma unroll
      for (int t = 0; t < NQ; ++t) {
        float mx = fmaxf(fmaxf(s[t][0], s[t][1]), fmaxf(fmaxf(s[t][2], s[t][3]), mcur[QO + t]));
        __hip_atomic_fetch_max((LDS3 float*)(uintptr_t)(mb + (unsigned)((QO + t) * 64)), mx,
                               __ATOMIC_RELAXED, __HIP_MEMORY_SCOPE_WORKGROUP);
      }
    };
    float mn[NM];
    auto ld_max = [&](int par) {
      const unsigned mb = addv(mofs, par * SQ * 4);
#pragma unroll
      for (int t = 0; t < NM; ++t) mn[t] = *(const LDS3 float*)(uintptr_t)(mb + (unsigned)(t * 64));
    };
    auto softmax = [&](int par) {
#pragma unroll
      for (int t = 0; t < NM; ++t) {
        alpha[t] = __builtin_amdgcn_exp2f((mcur[t] - mn[t]) * cs);
        mcur[t] = mn[t];
        PIN(alpha[t]);
      }
      const unsigned pwb = addv(pw, par * PBUF);
#pragma unroll
      for (int t = 0; t < NQ; ++t) {
        float mc = -mcur[QO + t] * cs;
        float p0 = __builtin_amdgcn_exp2f(fmaf(s[t][0], cs, mc));
        float p1 = __builtin_amdgcn_exp2f(fmaf(s[t][1], cs, mc));
        float p2 = __builtin_amdgcn_exp2f(fmaf(s[t][2], cs, mc));
        float p3 = __builtin_amdgcn_exp2f(fmaf(s[t][3], cs, mc));
        lsum[t] = fmaf(lsum[t], alpha[QO + t], (p0 + p1) + (p2 + p3));
        PIN(lsum[t]);
        *(LDS3 u32x2*)(uintptr_t)(pwb + (unsigned)(t * 2048)) = pack4(p0, p1, p2, p3);
      }
    };
    auto rescale = [&]() {
      asm volatile("s_nop 7\n\ts_nop 3" ::: "memory");
#pragma unroll
      for (int t = 0; t < NP; ++t)
#pragma unroll
        for (int i = 0; i < 3; ++i) {
          f32x2 lo = __builtin_shufflevector(acc[t][i], acc[t][i], 0, 1), hi = __builtin_shufflevector(acc[t][i], acc[t][i], 2, 3);
          f32x2 al = {alpha[(PT0 - MT0) + t], alpha[(PT0 - MT0) + t]};
          asm volatile("v_pk_mul_f32 %0, %0, %1 op_sel_hi:[1,0]" : "+v"(lo) : "v"(al));
          asm volatile("v_pk_mul_f32 %0, %0, %1 op_sel_hi:[1,0]" : "+v"(hi) : "v"(al));
          acc[t][i] = __builtin_shufflevector(lo, hi, 0, 1, 2, 3);
        }
      asm volatile("s_nop 1" ::: "memory");
    };
    constexpr int RING = G == 0 ? 3 : 4;
    constexpr int TAIL = G == 0 ? 0 : 3;
    auto pv_pre = [&](int unit, int par) {
#pragma unroll
      for (int i = 0; i < 3; ++i) {
        unsigned va = addv(vofs[i], unit * UNIT);
        fa[0][i] = lds_tr2(va, va + 1536);
      }
      unsigned pa = addv(pofs0, par * PBUF);
      fr[0] = lds_b128(pa);
      fr[1] = lds_b128(pa + 2048);
    };
    auto pv_main = [&](int unit, int par, int mpar) {
      unsigned pa = addv(pofs0, par * PBUF), pa1 = pa ^ 64u;
#pragma unroll
      for (int st = 0; st < 2 * NP - TAIL; ++st) {
        if (st + 2 < 2 * NP) fr[(st + 2) % RING] = lds_b128(((st + 2) >= NP ? pa1 : pa) + ((st + 2) % NP) * 2048);
        if (TAIL && st == 2 * NP - TAIL - 1) fr[(st + 3) % RING] = lds_b128(pa1 + ((st + 3) % NP) * 2048);
        if (st == 1) {
#pragma unroll
          for (int i = 0; i < 3; ++i) {
            unsigned va = addv(vofs[i], unit * UNIT) + 12288;
            fa[1][i] = lds_tr2(va, va + 1536);
          }
        }
        if (st == NP + 1 && mpar >= 0) ld_max(mpar);
        SB();
#pragma unroll
        for (int i = 0; i < 3; ++i) acc[st % NP][i] = MFMA(fa[st / NP][i], fr[st % RING], acc[st % NP][i]);
        SB();
      }
    };
    auto pv_tail = [&]() {
#pragma unroll
      for (int st = 2 * NP - TAIL; st < 2 * NP; ++st)
#pragma unroll
        for (int i = 0; i < 3; ++i) acc[st % NP][i] = MFMA(fa[1][i], fr[st % RING], acc[st % NP][i]);
    };

    qk_pre(0);
    qk(0, 0);
    pub_max(0);
    BAR();
    ld_max(0);
    qk_pre(1);
    issue_kv(kb + 3 * UNIT, 0);
    SB();
    softmax(0);
    SB();
    qk(1, 1);
    pub_max(1);
    WAIT_VM(6);
    BAR();
    int uv = 2;
    if (G == 1) {
#pragma unroll
      for (int i = 0; i < 3; ++i) fa[1][i] = __builtin_bit_cast(bf16x8, zero_acc());
#pragma unroll
      for (int i = 0; i < 4; ++i) fr[i] = __builtin_bit_cast(bf16x8, zero_acc());
    }
    for (int c = 1; c <= 6; ++c) {
      int uk = uv == 4 ? 0 : uv + 1;
      int u3 = uv >= 2 ? uv - 2 : uv + 3;
      int u4 = uv >= 1 ? uv - 1 : uv + 4;
      auto issue = [&]() {
        if (c <= 5) {
          if (c >= 2) issue_kv(kb + (size_t)(c + 2) * UNIT, u3);
          issue_kv(vb + (size_t)(c + 1) * UNIT, u4);
        } else {
          issue_kv(vb + (size_t)7 * UNIT, u3);
          if (G == 1 && it == 0) { mk_dofs(); issue_unit(A.k[1] + kvoff + UNIT, u4); }
        }
      };
      if (G == 0) {
        pv_pre(uv, (c - 1) & 1);
        SB();
        issue();
        rescale();
        SB();
        pv_main(uv, (c - 1) & 1, c & 1);
        qk_pre(uk);
        SB();
        softmax(c & 1);
        SB();
        qk(uk, (c + 1) & 1);
        pub_max((c + 1) & 1);
        WAIT_VM(3);
      } else {
        pv_tail();
        SB();
        ld_max(c & 1);
        issue();
        SB();
        rescale();
        qk_pre(uk);
        SB();
        softmax(c & 1);
        SB();
        qk(uk, (c + 1) & 1);
        pv_pre(uv, (c - 1) & 1);
        SB();
        pub_max((c + 1) & 1);
        SB();
        pv_main(uv, (c - 1) & 1, -1);
        if (c == 6 && it == 0) WAIT_VM(9); else WAIT_VM(3);
      }
      BAR();
      uv = uv >= 3 ? uv - 3 : uv + 2;
    }
    const int le = lane_id();
    if (G == 0) {
      pv_pre(4, 0);
      mk_dofs();
      if (it == 0) {
        const char* wn = A.wq[1] + (size_t)h * DH * 2;
        issue_unit(wn, 2);
        issue_unit(wn + (size_t)64 * NH * DH * 2, 3);
      }
      rescale();
      SB();
      pv_main(4, 0, 1);
      softmax(1);
    } else {
      pv_tail();
      SB();
      ld_max(1);
      SB();
      rescale();
      pv_pre(4, 0);
      SB();
      softmax(1);
      SB();
      pv_main(4, 0, -1);
    }
#pragma unroll
    for (int t = 0; t < NQ; ++t) {
      float l = lsum[t];
      l += __builtin_bit_cast(float, __builtin_amdgcn_ds_bpermute((le ^ 16) << 2, __builtin_bit_cast(int, l)));
      l += __builtin_bit_cast(float, __builtin_amdgcn_ds_bpermute((le ^ 32) << 2, __builtin_bit_cast(int, l)));
      if (le < 16) *(LDS3 float*)(uintptr_t)(L_L + (unsigned)(cg * SQ * 4 + (16 * (QT0 + t) + le) * 4)) = l;
    }
    if (G == 0) { if (it == 0) WAIT_VM(12); else WAIT_VM(0); }
    else { if (it == 0) WAIT_VM(6); else WAIT_VM(0); }
    BAR();
    if (G == 0) {
      pv_pre(0, 1);
      if (it == 0) {
        issue_x(A.x[1] + (size_t)b * SQ * DM * 2, 0);
        issue_unit(A.wq[1] + (size_t)h * DH * 2 + (size_t)128 * NH * DH * 2, 4);
      }
      rescale();
      SB();
      pv_main(0, 1, -1);
    } else {
      pv_tail();
      pv_pre(0, 1);
      SB();
      rescale();
      SB();
      pv_main(0, 1, -1);
      pv_tail();
    }
    {
      char* ob = A.ows + (size_t)((it * NB + b) * NH + h) * OWS_ITEM + le * 16;
      const int ufull = (3 * cg + 1) >> 1;
      const int uhalf = (cg & 1) ? (3 * cg) >> 1 : (3 * cg + 2) >> 1;
#pragma unroll
      for (int t = 0; t < NP; ++t) {
        unsigned la = L_L + (unsigned)((16 * (PT0 + t) + (le & 15)) * 4);
        float l = (*(const LDS3 float*)(uintptr_t)la + *(const LDS3 float*)(uintptr_t)(la + SQ * 4)) +
                  (*(const LDS3 float*)(uintptr_t)(la + 2 * SQ * 4) + *(const LDS3 float*)(uintptr_t)(la + 3 * SQ * 4));
        float inv = 1.0f / l;
        u32x2 o[3];
#pragma unroll
        for (int i = 0; i < 3; ++i) o[i] = pack4(acc[t][i][0] * inv, acc[t][i][1] * inv, acc[t][i][2] * inv, acc[t][i][3] * inv);
        u32x4 full; u32x2 half;
        if (cg & 1) { full = (u32x4){o[1][0], o[1][1], o[2][0], o[2][1]}; half = o[0]; }
        else        { full = (u32x4){o[0][0], o[0][1], o[1][0], o[1][1]}; half = o[2]; }
        *(u32x4*)(ob + (ufull >> 1) * PBUF + (PT0 + t) * 2048 + (ufull & 1) * 1024) = full;
        *(u32x2*)(ob + (uhalf >> 1) * PBUF + (PT0 + t) * 2048 + (uhalf & 1) * 1024 + (cg & 1) * 8) = half;
      }
    }
    if (G == 0 && it == 0) WAIT_VM(14);
    BAR();
  }
}

__global__ __launch_bounds__(512, 2) void k1_attn(K1Args A) {
  extern __shared__ __attribute__((aligned(16))) char shm[];
  const int w = __builtin_amdgcn_readfirstlane(threadIdx.x >> 6);
  const int xcd = blockIdx.x & 7, idx = blockIdx.x >> 3;
  const int h = xcd * 4 + (idx & 3), b = idx >> 2;
  if (w < 4) k1_body<0>(A, w, b, h);
  else k1_body<1>(A, w - 4, b, h);
}

constexpr int K2_STAGE = PBUF + 64 * 320;
constexpr int K2_NST = 4;
constexpr int K2_CH = 24;
struct K2Args { const char* wo[2]; const char* ows; float* part; };

template <int NR, int NC>
__device__ __forceinline__ void k2_body(const K2Args& A, int w, int lane, int rt0, int ct0, int br, int b, int nb, int sp) {
  STAMP_INIT();
  const int g = lane >> 4;
  unsigned dofs[5]; int dlds[5]; bool isw[5];
#pragma unroll
  for (int i = 0; i < 5; ++i) {
    int p = w + 8 * i; if (p >= 38) p -= 2;
    isw[i] = p >= 18;
    dlds[i] = p * 1024;
    if (p < 18) dofs[i] = (unsigned)(p * 1024 + lane * 16);
    else {
      int s = 64 * (p - 18) + lane, r = s / 20, c = s - r * 20;
      dofs[i] = (unsigned)(r * (DM * 2) + ((c ^ (((r >> 2) & 1) << 1)) << 4));
    }
  }
  const char* ob = A.ows + ((size_t)(br * NB + b) * NH * 3 + (size_t)sp * K2_CH) * PBUF;
  const char* wb = A.wo[br] + (size_t)sp * K2_CH * 64 * DM * 2 + nb * 320;
  auto issue = [&](int ci) {
    unsigned base = (unsigned)((ci & 3) * K2_STAGE);
#pragma unroll
    for (int i = 0; i < 5; ++i)
      dma16(base + (unsigned)dlds[i], dofs[i], isw[i] ? wb + (size_t)ci * 64 * DM * 2 : ob + (size_t)ci * PBUF);
  };
  unsigned aofs[NC];
  {
    const int q = (lane >> 2) & 3, p = lane & 3;
#pragma unroll
    for (int i = 0; i < NC; ++i) aofs[i] = PBUF + (unsigned)((4 * g + q) * 320 + (((ct0 + i) ^ (g & 1)) << 5) + p * 8);
  }
  const unsigned bofs = (unsigned)(rt0 * 2048 + lane * 16);
  f32x4 acc[NR][NC];
#pragma unroll
  for (int t = 0; t < NR; ++t)
#pragma unroll
    for (int i = 0; i < NC; ++i) acc[t][i] = (f32x4){0.f, 0.f, 0.f, 0.f};
  bf16x8 fa[2][NC], fb[2][NR];
  auto ld_set = [&](int ks, unsigned base) {
#pragma unroll
    for (int i = 0; i < NC; ++i) fa[ks][i] = lds_tr2(base + aofs[i] + ks * 10240, base + aofs[i] + ks * 10240 + 5120);
#pragma unroll
    for (int t = 0; t < NR; ++t) fb[ks][t] = lds_b128(base + bofs + t * 2048 + ks * 1024);
  };
  auto mm_set = [&](int ks) {
#pragma unroll
    for (int t = 0; t < NR; ++t)
#pragma unroll
      for (int i = 0; i < NC; ++i) acc[t][i] = MFMA(fa[ks][i], fb[ks][t], acc[t][i]);
  };
  issue(0); issue(1); issue(2);
  WAIT_VM(10);
  BAR();
  for (int ci = 0; ci < K2_CH; ++ci) {
    unsigned base = (unsigned)((ci & 3) * K2_STAGE);
    ld_set(0, base);
    SB();
    if (ci + 3 < K2_CH) issue(ci + 3);
    SB();
    if (ci > 0) mm_set(1);
    SB();
    ld_set(1, base);
    SB();
    mm_set(0);
    SB();
    if (ci < K2_CH - 3) WAIT_VM(10); else if (ci == K2_CH - 3) WAIT_VM(5); else WAIT_VM(0);
    BAR();
  }
  mm_set(1);
  float* pb = A.part + ((size_t)(((br * NB + b) * 4 + nb) * 4 + sp) * 90) * 256 + lane * 4;
#pragma unroll
  for (int t = 0; t < NR; ++t)
#pragma unroll
    for (int i = 0; i < NC; ++i) *(f32x4*)(pb + ((rt0 + t) * 10 + ct0 + i) * 256) = acc[t][i];
}

__global__ __launch_bounds__(512, 2) void k2_oproj(K2Args A) {
  extern __shared__ __attribute__((aligned(16))) char shm[];
  const int w = __builtin_amdgcn_readfirstlane(threadIdx.x >> 6);
  const int lane = threadIdx.x & 63;
  const int xcd = blockIdx.x & 7, idx = blockIdx.x >> 3;
  const int br = xcd >> 2, sp = xcd & 3, b = idx >> 2, nb = idx & 3;
  const int cg = w & 3;
  if (w < 2) k2_body<5, 3>(A, w, lane, 0, 3 * cg, br, b, nb, sp);
  else if (w < 4) k2_body<5, 2>(A, w, lane, 0, 2 * cg + 2, br, b, nb, sp);
  else if (w < 6) k2_body<4, 2>(A, w, lane, 5, 2 * cg, br, b, nb, sp);
  else k2_body<4, 3>(A, w, lane, 5, 3 * cg - 2, br, b, nb, sp);
}

__global__ __launch_bounds__(256) void k3_reduce(const float* __restrict__ part, char* __restrict__ out) {
  int gid = blockIdx.x * 256 + threadIdx.x;
  int lane = gid & 63, f = (gid >> 6) % 90, tile = (gid >> 6) / 90;
  const float* p = part + ((size_t)tile * 4 * 90 + f) * 256 + lane * 4;
  f32x4 s = *(const f32x4*)p;
  s += *(const f32x4*)(p + 90 * 256);
  s += *(const f32x4*)(p + 2 * 90 * 256);
  s += *(const f32x4*)(p + 3 * 90 * 256);
  int nb = tile & 3, b = (tile >> 2) & 7, br = tile >> 5;
  int rt = f / 10, ct = f - rt * 10;
  int q = rt * 16 + (lane & 15), n = br * DM + nb * 160 + ct * 16 + (lane >> 4) * 4;
  *(u32x2*)(out + ((size_t)(b * SQ + q) * (2 * DM) + n) * 2) = pack4(s[0], s[1], s[2], s[3]);
}

extern "C" void kernel_launch(void* const* d_in, const int* in_sizes, int n_in, void* d_out, int out_size,
                              void* d_ws, size_t ws_size, hipStream_t stream) {
  (void)in_sizes; (void)n_in; (void)out_size; (void)ws_size;
  K1Args a1;
  for (int i = 0; i < 2; ++i) {
    a1.x[i] = (const char*)d_in[5 * i + 0];
    a1.k[i] = (const char*)d_in[5 * i + 1];
    a1.v[i] = (const char*)d_in[5 * i + 2];
    a1.wq[i] = (const char*)d_in[5 * i + 3];
  }
  a1.ows = (char*)d_ws;
  K2Args a2;
  a2.wo[0] = (const char*)d_in[4];
  a2.wo[1] = (const char*)d_in[9];
  a2.ows = (const char*)d_ws;
  a2.part = (float*)((char*)d_ws + OWS_BYTES);
  (void)hipFuncSetAttribute((const void*)k1_attn, hipFuncAttributeMaxDynamicSharedMemorySize, L_TOT1);
  (void)hipFuncSetAttribute((const void*)k2_oproj, hipFuncAttributeMaxDynamicSharedMemorySize, K2_NST * K2_STAGE);
  k1_attn<<<256, 512, L_TOT1, stream>>>(a1);
  k2_oproj<<<256, 512, K2_NST * K2_STAGE, stream>>>(a2);
  k3_reduce<<<64 * 90 * 64 / 256, 256, 0, stream>>>(a2.part, (char*)d_out);
}
```

```cpp
#include <hip/hip_runtime.h>
#include <hip/hip_bf16.h>
#include <stdint.h>
#include <type_traits>

typedef __attribute__((ext_vector_type(4))) short s16x4;
typedef __attribute__((ext_vector_type(8))) short s16x8;
typedef __attribute__((ext_vector_type(8))) __bf16 bf16x8;
typedef __attribute__((ext_vector_type(4))) __bf16 bf16x4;
typedef __attribute__((ext_vector_type(4))) float f32x4;
typedef __attribute__((ext_vector_type(2))) float f32x2;
typedef __attribute__((ext_vector_type(2))) unsigned u32x2;
typedef __attribute__((ext_vector_type(4))) unsigned u32x4;
#define LDS3 __attribute__((address_space(3)))

constexpr int NB = 8, SQ = 144, DM = 640, NH = 32, SK = 512, DH = 192;
constexpr int UNIT = 24576;
constexpr int PBUF = 18432;
constexpr int L_PB = 5 * UNIT;
constexpr int L_M = L_PB + 2 * PBUF;
constexpr int L_L = L_M + 2 * SQ * 4;
constexpr int L_F = L_L + 4 * SQ * 4;
constexpr int L_TOT1 = L_F + 16;
constexpr int L_Q = 4 * UNIT;
constexpr size_t OWS_ITEM = 55296;
constexpr size_t OWS_BYTES = 512 * OWS_ITEM;

#define WAIT_VM(n) asm volatile("s_waitcnt vmcnt(" #n ")" ::: "memory")
#define STAMP_INIT() do {} while (0)
#define STAMP() do {} while (0)
#define BAR() do { asm volatile("" ::: "memory"); __builtin_amdgcn_s_waitcnt(0xC07F); __builtin_amdgcn_s_barrier(); asm volatile("" ::: "memory"); STAMP(); } while (0)

__device__ __forceinline__ void dma16(unsigned lds, unsigned voff, const char* base) {
  asm volatile("s_mov_b32 m0, %0\n\ts_nop 0\n\tglobal_load_lds_dwordx4 %1, %2"
               :: "s"(lds), "v"(voff), "s"(base) : "memory");
}
__device__ __forceinline__ bf16x8 lds_b128(unsigned a) {
  return *(const LDS3 bf16x8*)(uintptr_t)a;
}
__device__ __forceinline__ bf16x8 lds_tr2(unsigned a0, unsigned a1) {
  s16x4 x = __builtin_amdgcn_ds_read_tr16_b64_v4i16((LDS3 s16x4*)(uintptr_t)a0);
  s16x4 y = __builtin_amdgcn_ds_read_tr16_b64_v4i16((LDS3 s16x4*)(uintptr_t)a1);
  s16x8 z = __builtin_shufflevector(x, y, 0, 1, 2, 3, 4, 5, 6, 7);
  return __builtin_bit_cast(bf16x8, z);
}
__device__ __forceinline__ u32x2 pack4(float a, float b, float c, float d) {
  bf16x4 p; p[0] = (__bf16)a; p[1] = (__bf16)b; p[2] = (__bf16)c; p[3] = (__bf16)d;
  return __builtin_bit_cast(u32x2, p);
}
__device__ __forceinline__ int lane_id() {
  int r; asm volatile("v_mbcnt_lo_u32_b32 %0, -1, 0\n\tv_mbcnt_hi_u32_b32 %0, -1, %0" : "=v"(r)); return r;
}
__device__ __forceinline__ unsigned addv(unsigned v, int sc) {
  unsigned r; asm volatile("v_add_u32 %0, %1, %2" : "=v"(r) : "s"(sc), "v"(v)); return r;
}
__device__ __forceinline__ f32x4 zero_acc() {
  float z; asm volatile("v_mov_b32 %0, 0" : "=v"(z)); return (f32x4){z, z, z, z};
}
#if defined(__HIP_DEVICE_COMPILE__)
#define NO_PK __attribute__((target("no-packed-fp32-ops")))
#else
#define NO_PK
#endif
#define PIN(x) asm volatile("" : "+v"(x))
#define MFMA(a, b, c) __builtin_amdgcn_mfma_f32_16x16x32_bf16((a), (b), (c), 0, 0, 0)

struct K1Args {
  const char* x[2]; const char* k[2]; const char* v[2]; const char* wq[2];
  char* ows;
};

#define SB() __builtin_amdgcn_sched_barrier(0)
__device__ __forceinline__ void dma16x3(unsigned lds, unsigned v0, unsigned v1, unsigned v2, const char* base) {
  asm volatile("s_mov_b32 m0, %0\n\ts_nop 0\n\tglobal_load_lds_dwordx4 %1, %4\n\t"
               "global_load_lds_dwordx4 %2, %4 offset:1024\n\tglobal_load_lds_dwordx4 %3, %4 offset:2048"
               :: "s"(lds), "v"(v0), "v"(v1), "v"(v2), "s"(base) : "memory");
}

template <int OFF>
__device__ __forceinline__ void dma16o(unsigned lds, unsigned v, const char* base) {
  asm volatile("s_mov_b32 m0, %0\n\ts_nop 0\n\tglobal_load_lds_dwordx4 %1, %2 offset:%3"
               :: "s"(lds), "v"(v), "s"(base), "n"(OFF) : "memory");
}
__device__ __forceinline__ void dma16j(int j, unsigned lds, unsigned v, const char* base) {
  if (j == 0) dma16o<0>(lds, v, base); else if (j == 1) dma16o<1024>(lds, v, base); else dma16o<2048>(lds, v, base);
}
__device__ __forceinline__ float max3f(float a, float b, float c) { return fmaxf(fmaxf(a, b), c); }

template <int G>
__device__ __forceinline__ void k1_body(const K1Args& A, int cg, int b, int h) {
  const int lane = lane_id();
  STAMP_INIT();
  constexpr int NQ = G == 0 ? 5 : 4, QT0 = G == 0 ? 0 : 5;
  constexpr int NP = G == 0 ? 4 : 5, PT0 = G == 0 ? 0 : 4;
  constexpr int NM = 5;
  constexpr int MT0 = G == 0 ? 0 : 4;
  constexpr int QO = QT0 - MT0;
  const float cs = 0.07216878364870322f * 1.4426950408889634f;
  const int li = lane & 15, g = lane >> 4;
  const unsigned fv2 = (unsigned)((((li >> 1) & 1) | ((li >> 2) & 2)) << 1);
  const unsigned fk = (unsigned)(li >> 1);
  const unsigned kofs0 = (unsigned)((16 * cg + li) * 384) + (((unsigned)g ^ fv2) << 4);
  const unsigned pofs0 = L_PB + (unsigned)((16 * PT0 + li) * 128) + (((unsigned)g ^ fk) << 4);
  unsigned vofs[3];
  {
    const int q = (lane >> 2) & 3, p = lane & 3;
    const unsigned fv = (unsigned)(((q >> 1) & 1) | ((g & 1) << 1));
#pragma unroll
    for (int i = 0; i < 3; ++i) {
      unsigned t = (unsigned)(3 * cg + i);
      vofs[i] = (unsigned)((8 * g + q) * 384) + ((t ^ fv) << 5) + (unsigned)(p * 8);
    }
  }
  const unsigned pw = L_PB + (unsigned)((16 * QT0 + li) * 128) + ((((unsigned)(2 * cg + (g >> 1))) ^ fk) << 4) + (unsigned)((g & 1) * 8);
  const unsigned mofs = L_M + (unsigned)((16 * MT0 + li) * 4);
  unsigned dofs[6];
  unsigned kvo[3];
  unsigned xofs = 0;
  auto mk_dofs = [&]() {
    const int ln = lane_id();
#pragma unroll
    for (int j = 0; j < 6; ++j) {
      int s = 64 * j + ln, r = (s * 2731) >> 16, c = s - r * 24;
      int f = (((r >> 1) & 1) | ((r >> 2) & 2)) << 1;
      int ch = c ^ f;
      dofs[j] = (unsigned)((16 * cg + r) * (G == 0 ? NH * DH * 2 : 384) + ch * 16 - (j % 3) * 1024);
    }
    if (G == 0) xofs = (unsigned)((ln >> 3) * (DM * 2) + (((ln & 7) ^ ((4 * (cg & 1) + (ln >> 4)) & 7)) << 4));
  };
  auto mk_kvo = [&]() {
    const int ln = lane_id();
#pragma unroll
    for (int j = 0; j < 3; ++j) {
      int s = 64 * j + ln, r = (s * 2731) >> 16, c = s - r * 24;
      kvo[j] = (unsigned)((16 * cg + r) * 384 + (c ^ (((r >> 1) & 1) << 1)) * 16 - j * 1024);
    }
  };
  mk_dofs();

  const size_t kvoff = (size_t)(b * NH + h) * SK * DH * 2;
  auto issue_unit = [&](const char* src, int unit) {
    dma16x3((unsigned)(unit * UNIT + cg * 6144), dofs[0], dofs[1], dofs[2], src);
    dma16x3((unsigned)(unit * UNIT + cg * 6144 + 3072), dofs[3], dofs[4], dofs[5], src);
  };
  auto issue_kv = [&](const char* src, int unit) {
    if (G == 0) dma16x3((unsigned)(unit * UNIT + cg * 6144), kvo[0], kvo[1], kvo[2], src);
    else dma16x3((unsigned)(unit * UNIT + cg * 6144 + 3072), dofs[3], dofs[4], dofs[5], src);
  };
  auto unit1 = [&](const char* src, int unit, int j) {
    dma16j(j % 3, (unsigned)(unit * UNIT + cg * 6144 + (j / 3) * 3072), dofs[j], src);
  };
  auto kv1 = [&](const char* src, int unit, int j) {
    if (G == 0) dma16j(j, (unsigned)(unit * UNIT + cg * 6144), kvo[j], src);
    else dma16j(j, (unsigned)(unit * UNIT + cg * 6144 + 3072), dofs[3 + j], src);
  };
  auto x1 = [&](const char* src, int xo, int i) {
    int p = cg + 4 * i; if (p > 17) p -= 2;
    dma16((unsigned)(L_PB + xo + p * 1024), xofs, src + (size_t)p * 8 * DM * 2);
  };
  auto issue_x = [&](const char* src, int pb) {
#pragma unroll
    for (int i = 0; i < 5; ++i) {
      int p = cg + 4 * i; if (p > 17) p -= 2;
      dma16((unsigned)(L_PB + pb * PBUF + p * 1024), xofs, src + (size_t)p * 8 * DM * 2);
    }
  };

  if (G == 1) __builtin_amdgcn_s_setprio(1);
  if (G == 0) {
    const char* xb = A.x[0] + (size_t)b * SQ * DM * 2;
    const char* wb = A.wq[0] + (size_t)h * DH * 2;
    issue_unit(wb, 2);
    issue_x(xb, 0);
    issue_unit(wb + (size_t)64 * NH * DH * 2, 3);
    issue_unit(wb + (size_t)128 * NH * DH * 2, 4);
    WAIT_VM(12);
  } else {
    issue_unit(A.k[0] + kvoff + UNIT, 1);
    if (cg == 0 && lane < 2) *(LDS3 int*)(uintptr_t)(L_F + lane * 4) = 0;
  }
  BAR();

  f32x4 acc[NP][3];
  bf16x8 fa[2][3], fb[2][NP], fr[4];
  auto qphase = [&](int km, int unit, int xo, bool mm, bool rd, auto&& slot) {
    unsigned pa = addv(pofs0, xo) ^ (km ? 0u : 64u);
#pragma unroll
    for (int m = 0; m < 3 * NP; ++m) {
      if (mm) acc[m / 3][m % 3] = MFMA(fa[km][m % 3], fb[km][m / 3], acc[m / 3][m % 3]);
      SB();
      if (rd) {
        if (m < 3) {
          unsigned va = addv(vofs[m], unit * UNIT) + (1 - km) * 12288;
          fa[1 - km][m] = lds_tr2(va, va + 1536);
        } else if (m < 3 + NP) fb[1 - km][m - 3] = lds_b128(pa + (m - 3) * 2048);
      }
      slot(m);
      SB();
    }
  };
  auto noslot = [&](int) {};

  for (int it = 0; it < 2; ++it) {
    const char* xb = A.x[it] + (size_t)b * SQ * DM * 2;
    const char* wb = A.wq[it] + (size_t)h * DH * 2;
    const char* kb = A.k[it] + kvoff;
    const char* vb = A.v[it] + kvoff;
#pragma unroll
    for (int t = 0; t < NP; ++t)
#pragma unroll
      for (int i = 0; i < 3; ++i) acc[t][i] = zero_acc();
    {
      int tid = G * 256 + cg * 64 + lane_id();
      if (tid < 2 * SQ) *(LDS3 float*)(uintptr_t)(L_M + tid * 4) = -__builtin_inff();
    }
    qphase(1, 2, 0, false, true, [&](int m) { if (G == 0 && m >= 3 && m < 8) x1(xb + 128, PBUF, m - 3); });
    qphase(0, 2, 0, true, true, [&](int m) { if (G == 0 && (m & 1) && m < 10) x1(xb + 256, -L_PB, m >> 1); });
    if (G == 0) WAIT_VM(5);
    BAR();
    int wu = 3;
#pragma unroll 1
    for (int j = 1; j <= 7; ++j) {
      const char* wn = wb + (size_t)(j + 2) * 64 * NH * DH * 2;
      const int un = wu == 2 ? 4 : wu - 1;
      const int xo = wu == 4 ? -L_PB : (wu - 2) * PBUF, xn = un == 4 ? -L_PB : (un - 2) * PBUF;
      qphase(1, wu, xo, true, true, [&](int m) { if (G == 0 && m < 10 && (m & 1)) x1(xb + (j + 2) * 128, xn, m >> 1); if (G == 0 && m == 11) unit1(wn, un, 0); });
      qphase(0, wu, xo, true, true, [&](int m) { if (G == 0 && !(m & 1) && m < 10) unit1(wn, un, 1 + (m >> 1)); });
      if (G == 0) WAIT_VM(11);
      BAR();
      wu = wu == 4 ? 2 : wu + 1;
    }
    qphase(1, 4, -L_PB, true, true, noslot);
    qphase(0, 4, -L_PB, true, true, noslot);
    if (G == 0) WAIT_VM(0); else mk_dofs();
    BAR();
    qphase(1, 2, 0, true, true, [&](int m) { if (G == 1 && (m & 1) && m < 12) unit1(kb, 0, m >> 1); });
    qphase(0, 2, 0, true, true, [&](int m) { if (G == 1 && (m & 1) && m < 12) unit1(kb + 2 * UNIT, 3, m >> 1); });
    BAR();
    if (G == 0) mk_kvo();
    qphase(1, 0, 0, true, false, [&](int m) { if (m % 3 == 1 && m < 9) kv1(vb, 2, m / 3); });
    const int lq = lane_id(), liq = lq & 15, gq = lq >> 4;
    const unsigned fkq = (unsigned)(liq >> 1);
#pragma unroll
    for (int t = 0; t < NP; ++t)
#pragma unroll
      for (int i = 0; i < 3; ++i) {
        unsigned ch = (unsigned)(2 * (3 * cg + i) + (gq >> 1));
        unsigned a = L_Q + (unsigned)((16 * (PT0 + t) + liq) * 384) + ((ch ^ fkq) << 4) + (unsigned)((gq & 1) * 8);
        *(LDS3 u32x2*)(uintptr_t)a = pack4(acc[t][i][0], acc[t][i][1], acc[t][i][2], acc[t][i][3]);
        acc[t][i] = zero_acc();
      }
    BAR();
    bf16x8 qf[NQ][6];
    {
      unsigned q0 = L_Q + (unsigned)((16 * QT0 + liq) * 384) + (((unsigned)gq ^ fkq) << 4);
#pragma unroll
      for (int t = 0; t < NQ; ++t)
#pragma unroll
        for (int ks = 0; ks < 6; ++ks) qf[t][ks] = lds_b128(((ks & 1) ? (q0 ^ 64u) : q0) + t * 6144 + (ks >> 1) * 128);
    }
    if (G == 1) WAIT_VM(9);
    BAR();
    issue_kv(vb + UNIT, 4);

    constexpr float THR = 8.0f;
    f32x4 s[NQ];
    const f32x4 zero4 = {0.f, 0.f, 0.f, 0.f};
    float mc[NM], lsum[NQ], alpha[NP];
    bf16x8 kf[3];
    int flg = 0;
    bool pend = false;
#pragma unroll
    for (int t = 0; t < NM; ++t) mc[t] = 0.f;
#pragma unroll
    for (int t = 0; t < NQ; ++t) lsum[t] = 0.f;
#pragma unroll
    for (int t = 0; t < NP; ++t) alpha[t] = 1.f;

    auto qk_pre = [&](int unit) {
      unsigned k0 = addv(kofs0, unit * UNIT);
      kf[0] = lds_b128(k0);
      kf[1] = lds_b128(k0 ^ 64u);
    };
    auto qk = [&](int unit, auto&& slot) {
      unsigned k0 = addv(kofs0, unit * UNIT), k1 = k0 ^ 64u;
#pragma unroll
      for (int ks = 0; ks < 6; ++ks) {
#pragma unroll
        for (int t = 0; t < NQ; ++t) {
          s[t] = MFMA(kf[ks % 3], qf[t][ks], ks == 0 ? zero4 : s[t]);
          SB();
          if (t == 0 && ks + 2 < 6) kf[(ks + 2) % 3] = lds_b128(((ks & 1) ? k1 : k0) + ((ks + 2) >> 1) * 128);
          if (t >= 1) slot(ks * 4 + t);
          SB();
        }
      }
    };
    auto check = [&](int par, int gen, bool first) {
      float mx = -__builtin_inff();
#pragma unroll
      for (int t = 0; t < NQ; ++t) {
#pragma unroll
        for (int e = 0; e < 4; ++e) s[t][e] = fmaf(s[t][e], cs, mc[QO + t]);
        mx = max3f(max3f(mx, s[t][0], s[t][1]), s[t][2], s[t][3]);
      }
      if (first || __builtin_amdgcn_ballot_w64(!(mx <= THR)) != 0) {
        const unsigned mb = addv(mofs, par * SQ * 4);
#pragma unroll
        for (int t = 0; t < NQ; ++t) {
          float y = fmaxf(fmaxf(s[t][0], s[t][1]), fmaxf(s[t][2], s[t][3])) - mc[QO + t];
          __hip_atomic_fetch_max((LDS3 float*)(uintptr_t)(mb + (unsigned)((QO + t) * 64)), y,
                                 __ATOMIC_RELAXED, __HIP_MEMORY_SCOPE_WORKGROUP);
        }
        *(volatile LDS3 int*)(uintptr_t)(L_F + par * 4) = gen;
      }
    };
    auto ld_flag = [&](int par) { flg = *(volatile LDS3 int*)(uintptr_t)(L_F + par * 4); };
    auto update = [&](int par, int gen, bool first) {
      if (first || __builtin_amdgcn_readfirstlane(flg) == gen) {
        const unsigned mb = addv(mofs, par * SQ * 4);
#pragma unroll
        for (int t = 0; t < NM; ++t) {
          float m = *(const LDS3 float*)(uintptr_t)(mb + (unsigned)(t * 64));
          float mn = first ? -m : fminf(mc[t], -m);
          float d = mn - mc[t];
          mc[t] = mn;
          float al = __builtin_amdgcn_exp2f(d);
          if (t >= QO && t < QO + NQ) {
#pragma unroll
            for (int e = 0; e < 4; ++e) s[t - QO][e] += d;
            if (!first) lsum[t - QO] *= al;
          }
          if (!first && t < NP) alpha[t] = al;
        }
        if (!first) pend = true;
      }
    };
    auto softmax = [&](int par, auto&& slot) {
      const unsigned pwb = addv(pw, par * PBUF);
#pragma unroll
      for (int t = 0; t < NQ; ++t) {
        float p0 = __builtin_amdgcn_exp2f(s[t][0]);
        float p1 = __builtin_amdgcn_exp2f(s[t][1]);
        SB();
        slot(t);
        SB();
        float p2 = __builtin_amdgcn_exp2f(s[t][2]);
        float p3 = __builtin_amdgcn_exp2f(s[t][3]);
        lsum[t] += (p0 + p1) + (p2 + p3);
        PIN(lsum[t]);
        *(LDS3 u32x2*)(uintptr_t)(pwb + (unsigned)(t * 2048)) = pack4(p0, p1, p2, p3);
        SB();
      }
    };
    auto rescale = [&]() {
      if (pend) {
        asm volatile("s_nop 7\n\ts_nop 7\n\ts_nop 3" ::: "memory");
#pragma unroll
        for (int t = 0; t < NP; ++t)
#pragma unroll
          for (int i = 0; i < 3; ++i) {
#pragma unroll
            for (int e = 0; e < 4; ++e) {
              float v = acc[t][i][e];
              asm volatile("v_mul_f32 %0, %0, %1" : "+v"(v) : "v"(alpha[t]));
              acc[t][i][e] = v;
            }
          }
        asm volatile("s_nop 1" ::: "memory");
        pend = false;
      }
    };
    auto ld_v = [&](int ks, int unit, int i) {
      unsigned va = addv(vofs[i], unit * UNIT) + ks * 12288;
      fa[ks][i] = lds_tr2(va, va + 1536);
    };
    auto rs = [&](int st, int sh) { return (st >= NP ? st + sh : st) & 3; };
    constexpr int SH = G == 0 ? 0 : 2;
    auto ld_p = [&](int par, int st, int sh = G == 0 ? 0 : 2) {
      unsigned pa = addv(pofs0, par * PBUF);
      fr[rs(st, sh)] = lds_b128((st >= NP ? pa ^ 64u : pa) + (st % NP) * 2048);
    };
    auto pv_steps = [&](int s0, int s1, int sh, auto&& slot) {
#pragma unroll
      for (int st = s0; st < s1; ++st)
#pragma unroll
        for (int i = 0; i < 3; ++i) {
          acc[st % NP][i] = MFMA(fa[st / NP][i], fr[rs(st, sh)], acc[st % NP][i]);
          SB();
          slot(st * 3 + i);
          SB();
        }
    };
    const int gen0 = it * 8 + 1;

    qk_pre(0);
    qk(0, noslot);
    check(0, gen0, true);
    BAR();
    qk_pre(1);
    update(0, gen0, true);
    SB();
    softmax(0, noslot);
    qk(1, noslot);
    check(1, gen0 + 1, false);
    WAIT_VM(3);
    BAR();
    int uv = 2;
    if (G == 1) {
#pragma unroll
      for (int i = 0; i < 3; ++i) fa[1][i] = __builtin_bit_cast(bf16x8, zero_acc());
#pragma unroll
      for (int i = 0; i < 4; ++i) fr[i] = __builtin_bit_cast(bf16x8, zero_acc());
    }
    const char* wnx = A.wq[1] + (size_t)h * DH * 2;
    auto interval = [&](int c, auto kind_c) {
      constexpr int KIND = decltype(kind_c)::value;
      constexpr bool LAST = KIND == 3;
      const int uk = uv == 4 ? 0 : uv + 1;
      const int u3 = uv >= 2 ? uv - 2 : uv + 3;
      const int u4 = uv >= 1 ? uv - 1 : uv + 4;
      const int pp = (c - 1) & 1;
      const char* src3 = KIND <= 1 ? kb + (size_t)(c + 2) * UNIT : vb + (size_t)7 * UNIT;
      const char* src4 = vb + (size_t)(c + 1) * UNIT;
      auto d3 = [&](int j) { if (KIND == 1 || KIND == 2) kv1(src3, u3, j); };
      auto d4 = [&](int j) { if (KIND <= 1) kv1(src4, u4, j); };
      if (G == 0) {
        ld_flag(c & 1);
        SB();
        ld_v(0, uv, 0); ld_p(pp, 0); ld_v(0, uv, 1); ld_v(0, uv, 2); ld_p(pp, 1);
        if (LAST) mk_dofs();
        SB();
        update(c & 1, gen0 + c, false);
        SB();
        softmax(c & 1, [&](int t) { if (LAST) { if (it == 0) unit1(wnx, 2, t); } else if (t < 3) d3(t); });
        pv_steps(0, 8, SH, [&](int m) {
          const int st = m / 3, i = m % 3;
          if (i == 0 && st + 2 < 8) ld_p(pp, st + 2);
          if (i == 1 && st < 3) ld_v(1, uv, st);
          if (!LAST && i == 1 && st >= 3 && st < 6) d4(st - 3);
          if (!LAST && m == 19) qk_pre(uk);
          if (LAST && it == 0 && i == 2 && st < 7) { if (st == 0) unit1(wnx, 2, 5); else unit1(wnx + (size_t)64 * NH * DH * 2, 3, st - 1); }
        });
        rescale();
        if (!LAST) {
          qk(uk, noslot);
          check((c + 1) & 1, gen0 + c + 1, false);
          WAIT_VM(3);
        }
      } else {
        ld_flag(c & 1);
        pv_steps(7, 10, SH, [&](int m) {
          if (m == 21) ld_v(0, uv, 0);
          if (m == 22) ld_p(pp, 0);
          if (m == 23) ld_v(0, uv, 1);
          if (m == 24) ld_v(0, uv, 2);
          if (m == 25) ld_p(pp, 1);
          if (m == 26) d3(0);
          if (m == 27) ld_p(pp, 2);
          if (m == 28) d3(1);
        });
        rescale();
        pv_steps(0, 5, SH, [&](int m) {
          if (m == 0) ld_p(pp, 3);
          if (m == 1) d3(2);
          if (m == 3) ld_p(pp, 4);
          if (!LAST && m == 10) qk_pre(uk);
        });
        update(c & 1, gen0 + c, false);
        SB();
        softmax(c & 1, [&](int t) { if (t < 3) d4(t); });
        if (KIND == 2 && it == 0) { mk_dofs(); issue_unit(A.k[1] + kvoff + UNIT, u4); }
        if (!LAST) {
          qk(uk, [&](int m) {
            if (m == 13) ld_v(1, uv, 0);
            if (m == 14) ld_p(pp, 5);
            if (m == 17) ld_v(1, uv, 1);
            if (m == 18) ld_v(1, uv, 2);
            if (m == 21) ld_p(pp, 6);
            if (m == 22) ld_p(pp, 7);
          });
          check((c + 1) & 1, gen0 + c + 1, false);
        } else {
          ld_v(1, uv, 0); ld_p(pp, 5); ld_v(1, uv, 1); ld_v(1, uv, 2); ld_p(pp, 6); ld_p(pp, 7);
        }
        SB();
        pv_steps(5, 7, SH, [&](int m) { if (m == 15) ld_p(pp, 8); if (m == 18) ld_p(pp, 9); });
        if (!LAST) { if (KIND == 2 && it == 0) WAIT_VM(9); else WAIT_VM(3); }
      }
      uv = uv >= 3 ? uv - 3 : uv + 2;
    };
#pragma unroll 1
    for (int c = 1; c <= 5; ++c) {
      interval(c, std::integral_constant<int, 1>{});
      BAR();
    }
    interval(6, std::integral_constant<int, 2>{});
    BAR();
    interval(7, std::integral_constant<int, 3>{});
    const int le = lane_id();
#pragma unroll
    for (int t = 0; t < NQ; ++t) {
      float l = lsum[t];
      l += __builtin_bit_cast(float, __builtin_amdgcn_ds_bpermute((le ^ 16) << 2, __builtin_bit_cast(int, l)));
      l += __builtin_bit_cast(float, __builtin_amdgcn_ds_bpermute((le ^ 32) << 2, __builtin_bit_cast(int, l)));
      if (le < 16) *(LDS3 float*)(uintptr_t)(L_L + (unsigned)(cg * SQ * 4 + (16 * (QT0 + t) + le) * 4)) = l;
    }
    if (G == 0) { if (it == 0) WAIT_VM(12); else WAIT_VM(0); }
    else { if (it == 0) WAIT_VM(6); else WAIT_VM(0); }
    BAR();
    if (G == 0) {
      ld_v(0, 0, 0); ld_p(1, 0); ld_v(0, 0, 1); ld_v(0, 0, 2); ld_p(1, 1);
      SB();
      if (it == 0) issue_x(A.x[1] + (size_t)b * SQ * DM * 2, 0);
      pv_steps(0, 8, SH, [&](int m) {
        const int st = m / 3, i = m % 3;
        if (i == 0 && st + 2 < 8) ld_p(1, st + 2);
        if (i == 1 && st < 3) ld_v(1, 0, st);
        if (it == 0 && i == 2 && st < 6) unit1(wnx + (size_t)128 * NH * DH * 2, 4, st);
      });
    } else {
      pv_steps(7, 10, SH, [&](int m) {
        if (m == 21) ld_v(0, 0, 0);
        if (m == 22) ld_p(1, 0);
        if (m == 23) ld_v(0, 0, 1);
        if (m == 24) ld_v(0, 0, 2);
        if (m == 25) ld_p(1, 1);
        if (m == 27) ld_p(1, 2);
      });
      rescale();
      pv_steps(0, 10, 0, [&](int m) {
        const int st = m / 3, i = m % 3;
        if (i == 0 && st + 3 < 10) ld_p(1, st + 3, 0);
        if (i == 1 && st < 3) ld_v(1, 0, st);
      });
    }
    {
      char* ob = A.ows + (size_t)((it * NB + b) * NH + h) * OWS_ITEM + le * 16;
      const int ufull = (3 * cg + 1) >> 1;
      const int uhalf = (cg & 1) ? (3 * cg) >> 1 : (3 * cg + 2) >> 1;
#pragma unroll
      for (int t = 0; t < NP; ++t) {
        unsigned la = L_L + (unsigned)((16 * (PT0 + t) + (le & 15)) * 4);
        float l = (*(const LDS3 float*)(uintptr_t)la + *(const LDS3 float*)(uintptr_t)(la + SQ * 4)) +
                  (*(const LDS3 float*)(uintptr_t)(la + 2 * SQ * 4) + *(const LDS3 float*)(uintptr_t)(la + 3 * SQ * 4));
        float inv = 1.0f / l;
        u32x2 o[3];
#pragma unroll
        for (int i = 0; i < 3; ++i) o[i] = pack4(acc[t][i][0] * inv, acc[t][i][1] * inv, acc[t][i][2] * inv, acc[t][i][3] * inv);
        u32x4 full; u32x2 half;
        if (cg & 1) { full = (u32x4){o[1][0], o[1][1], o[2][0], o[2][1]}; half = o[0]; }
        else        { full = (u32x4){o[0][0], o[0][1], o[1][0], o[1][1]}; half = o[2]; }
        *(u32x4*)(ob + (ufull >> 1) * PBUF + (PT0 + t) * 2048 + (ufull & 1) * 1024) = full;
        *(u32x2*)(ob + (uhalf >> 1) * PBUF + (PT0 + t) * 2048 + (uhalf & 1) * 1024 + (cg & 1) * 8) = half;
      }
    }
    if (G == 0 && it == 0) WAIT_VM(14);
    BAR();
  }
}

NO_PK __global__ __launch_bounds__(512, 2) void k1_attn(K1Args A) {
  extern __shared__ __attribute__((aligned(16))) char shm[];
  const int w = __builtin_amdgcn_readfirstlane(threadIdx.x >> 6);
  const int xcd = blockIdx.x & 7, idx = blockIdx.x >> 3;
  const int h = xcd * 4 + (idx & 3), b = idx >> 2;
  if (w < 4) k1_body<0>(A, w, b, h);
  else k1_body<1>(A, w - 4, b, h);
}

constexpr int K2_STAGE = PBUF + 64 * 320;
constexpr int K2_NST = 4;
constexpr int K2_CH = 24;
struct K2Args { const char* wo[2]; const char* ows; float* part; };

template <int NR, int NC>
__device__ __forceinline__ void k2_body(const K2Args& A, int w, int lane, int rt0, int ct0, int br, int b, int nb, int sp) {
  STAMP_INIT();
  const int g = lane >> 4;
  unsigned dofs[5]; int dlds[5]; bool isw[5];
#pragma unroll
  for (int i = 0; i < 5; ++i) {
    int p = w + 8 * i; if (p >= 38) p -= 2;
    isw[i] = p >= 18;
    dlds[i] = p * 1024;
    if (p < 18) dofs[i] = (unsigned)(p * 1024 + lane * 16);
    else {
      int s = 64 * (p - 18) + lane, r = s / 20, c = s - r * 20;
      dofs[i] = (unsigned)(r * (DM * 2) + ((c ^ (((r >> 2) & 1) << 1)) << 4));
    }
  }
  const char* ob = A.ows + ((size_t)(br * NB + b) * NH * 3 + (size_t)sp * K2_CH) * PBUF;
  const char* wb = A.wo[br] + (size_t)sp * K2_CH * 64 * DM * 2 + nb * 320;
  auto issue1 = [&](int ci, int i) {
    dma16((unsigned)((ci & 3) * K2_STAGE) + (unsigned)dlds[i], dofs[i], isw[i] ? wb + (size_t)ci * 64 * DM * 2 : ob + (size_t)ci * PBUF);
  };
  auto issue = [&](int ci) {
#pragma unroll
    for (int i = 0; i < 5; ++i) issue1(ci, i);
  };
  unsigned aofs[NC];
  {
    const int q = (lane >> 2) & 3, p = lane & 3;
#pragma unroll
    for (int i = 0; i < NC; ++i) aofs[i] = PBUF + (unsigned)((4 * g + q) * 320 + (((ct0 + i) ^ (g & 1)) << 5) + p * 8);
  }
  const unsigned bofs = (unsigned)(rt0 * 2048 + lane * 16);
  f32x4 acc[NR][NC];
#pragma unroll
  for (int t = 0; t < NR; ++t)
#pragma unroll
    for (int i = 0; i < NC; ++i) acc[t][i] = (f32x4){0.f, 0.f, 0.f, 0.f};
  bf16x8 fa[2][NC], fb[2][NR];
  auto ld_one = [&](int ks, unsigned base, int r) {
    if (r < NC) fa[ks][r] = lds_tr2(base + aofs[r] + ks * 10240, base + aofs[r] + ks * 10240 + 5120);
    else if (r < NC + NR) fb[ks][r - NC] = lds_b128(base + bofs + (r - NC) * 2048 + ks * 1024);
  };
  auto phase = [&](int km, unsigned base, bool rd, int cd, int d0, int nd) {
#pragma unroll
    for (int m = 0; m < NR * NC; ++m) {
      acc[m / NC][m % NC] = MFMA(fa[km][m % NC], fb[km][m / NC], acc[m / NC][m % NC]);
      SB();
      if (rd) ld_one(1 - km, base, m);
      constexpr int GAP = NR * NC >= 12 ? 4 : 3;
      if (cd >= 0 && m % GAP == 1 && m / GAP < nd) issue1(cd, d0 + m / GAP);
      SB();
    }
  };
  issue(0); issue(1); issue(2);
  WAIT_VM(10);
  BAR();
#pragma unroll
  for (int r = 0; r < NC + NR; ++r) ld_one(0, 0u, r);
  SB();
  issue(3);
  SB();
  phase(0, 0u, true, -1, 0, 0);
  WAIT_VM(10);
  BAR();
  for (int ci = 1; ci < K2_CH - 3; ++ci) {
    unsigned base = (unsigned)((ci & 3) * K2_STAGE);
    phase(1, base, true, ci + 3, 0, 3);
    phase(0, base, true, ci + 3, 3, 2);
    WAIT_VM(10);
    BAR();
  }
#pragma unroll
  for (int ci = K2_CH - 3; ci < K2_CH; ++ci) {
    unsigned base = (unsigned)((ci & 3) * K2_STAGE);
    phase(1, base, true, -1, 0, 0);
    phase(0, base, true, -1, 0, 0);
    if (ci == K2_CH - 3) { WAIT_VM(5); BAR(); } else if (ci == K2_CH - 2) { WAIT_VM(0); BAR(); }
  }
  phase(1, 0u, false, -1, 0, 0);
  float* pb = A.part + ((size_t)(((br * NB + b) * 4 + nb) * 4 + sp) * 90) * 256 + lane * 4;
#pragma unroll
  for (int t = 0; t < NR; ++t)
#pragma unroll
    for (int i = 0; i < NC; ++i) *(f32x4*)(pb + ((rt0 + t) * 10 + ct0 + i) * 256) = acc[t][i];
}

__global__ __launch_bounds__(512, 2) void k2_oproj(K2Args A) {
  extern __shared__ __attribute__((aligned(16))) char shm[];
  const int w = __builtin_amdgcn_readfirstlane(threadIdx.x >> 6);
  const int lane = threadIdx.x & 63;
  const int xcd = blockIdx.x & 7, idx = blockIdx.x >> 3;
  const int br = xcd >> 2, sp = xcd & 3, b = idx >> 2, nb = idx & 3;
  const int cg = w & 3;
  if (w < 2) k2_body<5, 3>(A, w, lane, 0, 3 * cg, br, b, nb, sp);
  else if (w < 4) k2_body<5, 2>(A, w, lane, 0, 2 * cg + 2, br, b, nb, sp);
  else if (w < 6) k2_body<4, 2>(A, w, lane, 5, 2 * cg, br, b, nb, sp);
  else k2_body<4, 3>(A, w, lane, 5, 3 * cg - 2, br, b, nb, sp);
}

__global__ __launch_bounds__(256) void k3_reduce(const float* __restrict__ part, char* __restrict__ out) {
  int gid = blockIdx.x * 256 + threadIdx.x;
  int lane = gid & 63, f = (gid >> 6) % 90, tile = (gid >> 6) / 90;
  const float* p = part + ((size_t)tile * 4 * 90 + f) * 256 + lane * 4;
  f32x4 s = *(const f32x4*)p;
  s += *(const f32x4*)(p + 90 * 256);
  s += *(const f32x4*)(p + 2 * 90 * 256);
  s += *(const f32x4*)(p + 3 * 90 * 256);
  int nb = tile & 3, b = (tile >> 2) & 7, br = tile >> 5;
  int rt = f / 10, ct = f - rt * 10;
  int q = rt * 16 + (lane & 15), n = br * DM + nb * 160 + ct * 16 + (lane >> 4) * 4;
  *(u32x2*)(out + ((size_t)(b * SQ + q) * (2 * DM) + n) * 2) = pack4(s[0], s[1], s[2], s[3]);
}

extern "C" void kernel_launch(void* const* d_in, const int* in_sizes, int n_in, void* d_out, int out_size,
                              void* d_ws, size_t ws_size, hipStream_t stream) {
  (void)in_sizes; (void)n_in; (void)out_size; (void)ws_size;
  K1Args a1;
  for (int i = 0; i < 2; ++i) {
    a1.x[i] = (const char*)d_in[5 * i + 0];
    a1.k[i] = (const char*)d_in[5 * i + 1];
    a1.v[i] = (const char*)d_in[5 * i + 2];
    a1.wq[i] = (const char*)d_in[5 * i + 3];
  }
  a1.ows = (char*)d_ws;
  K2Args a2;
  a2.wo[0] = (const char*)d_in[4];
  a2.wo[1] = (const char*)d_in[9];
  a2.ows = (const char*)d_ws;
  a2.part = (float*)((char*)d_ws + OWS_BYTES);
  (void)hipFuncSetAttribute((const void*)k1_attn, hipFuncAttributeMaxDynamicSharedMemorySize, L_TOT1);
  (void)hipFuncSetAttribute((const void*)k2_oproj, hipFuncAttributeMaxDynamicSharedMemorySize, K2_NST * K2_STAGE);
  k1_attn<<<256, 512, L_TOT1, stream>>>(a1);
  k2_oproj<<<256, 512, K2_NST * K2_STAGE, stream>>>(a2);
  k3_reduce<<<64 * 90 * 64 / 256, 256, 0, stream>>>(a2.part, (char*)d_out);
}
```

```cpp
#include <hip/hip_runtime.h>
#include <hip/hip_bf16.h>
#include <stdint.h>
#include <type_traits>

typedef __attribute__((ext_vector_type(4))) short s16x4;
typedef __attribute__((ext_vector_type(8))) short s16x8;
typedef __attribute__((ext_vector_type(8))) __bf16 bf16x8;
typedef __attribute__((ext_vector_type(4))) __bf16 bf16x4;
typedef __attribute__((ext_vector_type(4))) float f32x4;
typedef __attribute__((ext_vector_type(2))) float f32x2;
typedef __attribute__((ext_vector_type(2))) unsigned u32x2;
typedef __attribute__((ext_vector_type(4))) unsigned u32x4;
#define LDS3 __attribute__((address_space(3)))

constexpr int NB = 8, SQ = 144, DM = 640, NH = 32, SK = 512, DH = 192;
constexpr int UNIT = 24576;
constexpr int PBUF = 18432;
constexpr int L_PB = 5 * UNIT;
constexpr int L_M = L_PB + 2 * PBUF;
constexpr int L_L = L_M + 2 * SQ * 4;
constexpr int L_F = L_L + 4 * SQ * 4;
constexpr int L_PF = L_F + 16;
constexpr int L_TOT1 = L_PF + 256;
#define NPF 2
constexpr int L_Q = 4 * UNIT;
constexpr size_t OWS_ITEM = 55296;
constexpr size_t OWS_BYTES = 512 * OWS_ITEM;

#define WAIT_VM(n) asm volatile("s_waitcnt vmcnt(" #n ")" ::: "memory")
#define STAMP_INIT() do {} while (0)
#define STAMP() do {} while (0)
#define BAR() do { asm volatile("" ::: "memory"); __builtin_amdgcn_s_waitcnt(0xC07F); __builtin_amdgcn_s_barrier(); asm volatile("" ::: "memory"); STAMP(); } while (0)

__device__ __forceinline__ void dma16(unsigned lds, unsigned voff, const char* base) {
  asm volatile("s_mov_b32 m0, %0\n\ts_nop 0\n\tglobal_load_lds_dwordx4 %1, %2"
               :: "s"(lds), "v"(voff), "s"(base) : "memory");
}
__device__ __forceinline__ bf16x8 lds_b128(unsigned a) {
  return *(const LDS3 bf16x8*)(uintptr_t)a;
}
__device__ __forceinline__ bf16x8 lds_tr2(unsigned a0, unsigned a1) {
  s16x4 x = __builtin_amdgcn_ds_read_tr16_b64_v4i16((LDS3 s16x4*)(uintptr_t)a0);
  s16x4 y = __builtin_amdgcn_ds_read_tr16_b64_v4i16((LDS3 s16x4*)(uintptr_t)a1);
  s16x8 z = __builtin_shufflevector(x, y, 0, 1, 2, 3, 4, 5, 6, 7);
  return __builtin_bit_cast(bf16x8, z);
}
__device__ __forceinline__ u32x2 pack4(float a, float b, float c, float d) {
  bf16x4 p; p[0] = (__bf16)a; p[1] = (__bf16)b; p[2] = (__bf16)c; p[3] = (__bf16)d;
  return __builtin_bit_cast(u32x2, p);
}
__device__ __forceinline__ int lane_id() {
  int r; asm volatile("v_mbcnt_lo_u32_b32 %0, -1, 0\n\tv_mbcnt_hi_u32_b32 %0, -1, %0" : "=v"(r)); return r;
}
__device__ __forceinline__ unsigned addv(unsigned v, int sc) {
  unsigned r; asm volatile("v_add_u32 %0, %1, %2" : "=v"(r) : "s"(sc), "v"(v)); return r;
}
__device__ __forceinline__ f32x4 zero_acc() {
  float z; asm volatile("v_mov_b32 %0, 0" : "=v"(z)); return (f32x4){z, z, z, z};
}
#if defined(__HIP_DEVICE_COMPILE__)
#define NO_PK __attribute__((target("no-packed-fp32-ops")))
#else
#define NO_PK
#endif
#define PIN(x) asm volatile("" : "+v"(x))
#define MFMA(a, b, c) __builtin_amdgcn_mfma_f32_16x16x32_bf16((a), (b), (c), 0, 0, 0)

struct K1Args {
  const char* x[2]; const char* k[2]; const char* v[2]; const char* wq[2];
  char* ows;
};

#define SB() __builtin_amdgcn_sched_barrier(0)
__device__ __forceinline__ void dma16x3(unsigned lds, unsigned v0, unsigned v1, unsigned v2, const char* base) {
  asm volatile("s_mov_b32 m0, %0\n\ts_nop 0\n\tglobal_load_lds_dwordx4 %1, %4\n\t"
               "global_load_lds_dwordx4 %2, %4 offset:1024\n\tglobal_load_lds_dwordx4 %3, %4 offset:2048"
               :: "s"(lds), "v"(v0), "v"(v1), "v"(v2), "s"(base) : "memory");
}

template <int OFF>
__device__ __forceinline__ void dma16o(unsigned lds, unsigned v, const char* base) {
  asm volatile("s_mov_b32 m0, %0\n\ts_nop 0\n\tglobal_load_lds_dwordx4 %1, %2 offset:%3"
               :: "s"(lds), "v"(v), "s"(base), "n"(OFF) : "memory");
}
__device__ __forceinline__ void dma16j(int j, unsigned lds, unsigned v, const char* base) {
  if (j == 0) dma16o<0>(lds, v, base); else if (j == 1) dma16o<1024>(lds, v, base); else dma16o<2048>(lds, v, base);
}
__device__ __forceinline__ void warm(unsigned voff, const char* base) {
  asm volatile("s_mov_b32 m0, %0\n\ts_nop 0\n\tglobal_load_lds_dword %1, %2" :: "s"(L_PF), "v"(voff), "s"(base) : "memory");
}
__device__ __forceinline__ float max3f(float a, float b, float c) { return fmaxf(fmaxf(a, b), c); }

template <int G>
__device__ __forceinline__ void k1_body(const K1Args& A, int cg, int b, int h) {
  const int lane = lane_id();
  STAMP_INIT();
  constexpr int NQ = G == 0 ? 5 : 4, QT0 = G == 0 ? 0 : 5;
  constexpr int NP = G == 0 ? 4 : 5, PT0 = G == 0 ? 0 : 4;
  constexpr int NM = 5;
  constexpr int MT0 = G == 0 ? 0 : 4;
  constexpr int QO = QT0 - MT0;
  const float cs = 0.07216878364870322f * 1.4426950408889634f;
  const int li = lane & 15, g = lane >> 4;
  const unsigned fv2 = (unsigned)((((li >> 1) & 1) | ((li >> 2) & 2)) << 1);
  const unsigned fk = (unsigned)(li >> 1);
  const unsigned kofs0 = (unsigned)((16 * cg + li) * 384) + (((unsigned)g ^ fv2) << 4);
  const unsigned pofs0 = L_PB + (unsigned)((16 * PT0 + li) * 128) + (((unsigned)g ^ fk) << 4);
  unsigned vofs[3];
  {
    const int q = (lane >> 2) & 3, p = lane & 3;
    const unsigned fv = (unsigned)(((q >> 1) & 1) | ((g & 1) << 1));
#pragma unroll
    for (int i = 0; i < 3; ++i) {
      unsigned t = (unsigned)(3 * cg + i);
      vofs[i] = (unsigned)((8 * g + q) * 384) + ((t ^ fv) << 5) + (unsigned)(p * 8);
    }
  }
  const unsigned pw = L_PB + (unsigned)((16 * QT0 + li) * 128) + ((((unsigned)(2 * cg + (g >> 1))) ^ fk) << 4) + (unsigned)((g & 1) * 8);
  const unsigned mofs = L_M + (unsigned)((16 * MT0 + li) * 4);
  unsigned dofs[6];
  unsigned kvo[3];
  unsigned xofs = 0;
  auto mk_dofs = [&]() {
    const int ln = lane_id();
#pragma unroll
    for (int j = 0; j < 6; ++j) {
      int s = 64 * j + ln, r = (s * 2731) >> 16, c = s - r * 24;
      int f = (((r >> 1) & 1) | ((r >> 2) & 2)) << 1;
      int ch = c ^ f;
      dofs[j] = (unsigned)((16 * cg + r) * (G == 0 ? NH * DH * 2 : 384) + ch * 16 - (j % 3) * 1024);
    }
    if (G == 0) xofs = (unsigned)((ln >> 3) * (DM * 2) + (((ln & 7) ^ ((4 * (cg & 1) + (ln >> 4)) & 7)) << 4));
  };
  auto mk_kvo = [&]() {
    const int ln = lane_id();
#pragma unroll
    for (int j = 0; j < 3; ++j) {
      int s = 64 * j + ln, r = (s * 2731) >> 16, c = s - r * 24;
      kvo[j] = (unsigned)((16 * cg + r) * 384 + (c ^ (((r >> 1) & 1) << 1)) * 16 - j * 1024);
    }
  };
  mk_dofs();

  const size_t kvoff = (size_t)(b * NH + h) * SK * DH * 2;
  auto issue_unit = [&](const char* src, int unit) {
    dma16x3((unsigned)(unit * UNIT + cg * 6144), dofs[0], dofs[1], dofs[2], src);
    dma16x3((unsigned)(unit * UNIT + cg * 6144 + 3072), dofs[3], dofs[4], dofs[5], src);
  };
  auto issue_kv = [&](const char* src, int unit) {
    if (G == 0) dma16x3((unsigned)(unit * UNIT + cg * 6144), kvo[0], kvo[1], kvo[2], src);
    else dma16x3((unsigned)(unit * UNIT + cg * 6144 + 3072), dofs[3], dofs[4], dofs[5], src);
  };
  auto unit1 = [&](const char* src, int unit, int j) {
    dma16j(j % 3, (unsigned)(unit * UNIT + cg * 6144 + (j / 3) * 3072), dofs[j], src);
  };
  auto kv1 = [&](const char* src, int unit, int j) {
    if (G == 0) dma16j(j, (unsigned)(unit * UNIT + cg * 6144), kvo[j], src);
    else dma16j(j, (unsigned)(unit * UNIT + cg * 6144 + 3072), dofs[3 + j], src);
  };
  auto x1 = [&](const char* src, int xo, int i) {
    int p = cg + 4 * i; if (p > 17) p -= 2;
    dma16((unsigned)(L_PB + xo + p * 1024), xofs, src + (size_t)p * 8 * DM * 2);
  };
  auto issue_x = [&](const char* src, int pb) {
#pragma unroll
    for (int i = 0; i < 5; ++i) {
      int p = cg + 4 * i; if (p > 17) p -= 2;
      dma16((unsigned)(L_PB + pb * PBUF + p * 1024), xofs, src + (size_t)p * 8 * DM * 2);
    }
  };

  auto warm_wq = [&](const char* w) {
    const int ln = lane_id();
    if (b >= 1) {
#pragma unroll
      for (int k = 0; k < 6; ++k)
        if ((k & 3) == cg) {
          int idx = 64 * k + ln, r = (idx * 10923) >> 16, sg = idx - r * 6;
          warm((unsigned)(r * (NH * DH * 2) + sg * 64), w + (size_t)(b + 2) * 64 * NH * DH * 2);
        }
    }
  };
  if (G == 1) __builtin_amdgcn_s_setprio(1);
  if (G == 0) {
    const char* xb = A.x[0] + (size_t)b * SQ * DM * 2;
    const char* wb = A.wq[0] + (size_t)h * DH * 2;
    issue_unit(wb, 2);
    issue_x(xb, 0);
    issue_unit(wb + (size_t)64 * NH * DH * 2, 3);
    issue_unit(wb + (size_t)128 * NH * DH * 2, 4);
    WAIT_VM(12);
  } else {
    warm_wq(A.wq[0] + (size_t)h * DH * 2);
    issue_unit(A.k[0] + kvoff + UNIT, 1);
    if (cg == 0 && lane < 2) *(LDS3 int*)(uintptr_t)(L_F + lane * 4) = 0;
  }
  BAR();

  f32x4 acc[NP][3];
  bf16x8 fa[2][3], fb[2][NP], fr[4];
  auto qphase = [&](int km, int unit, int xo, bool mm, bool rd, auto&& slot) {
    unsigned pa = addv(pofs0, xo) ^ (km ? 0u : 64u);
#pragma unroll
    for (int m = 0; m < 3 * NP; ++m) {
      if (mm) acc[m / 3][m % 3] = MFMA(fa[km][m % 3], fb[km][m / 3], acc[m / 3][m % 3]);
      SB();
      if (rd) {
        if (m < 3) {
          unsigned va = addv(vofs[m], unit * UNIT) + (1 - km) * 12288;
          fa[1 - km][m] = lds_tr2(va, va + 1536);
        } else if (m < 3 + NP) fb[1 - km][m - 3] = lds_b128(pa + (m - 3) * 2048);
      }
      slot(m);
      SB();
    }
  };
  auto noslot = [&](int) {};

  for (int it = 0; it < 2; ++it) {
    const char* xb = A.x[it] + (size_t)b * SQ * DM * 2;
    const char* wb = A.wq[it] + (size_t)h * DH * 2;
    const char* kb = A.k[it] + kvoff;
    const char* vb = A.v[it] + kvoff;
#pragma unroll
    for (int t = 0; t < NP; ++t)
#pragma unroll
      for (int i = 0; i < 3; ++i) acc[t][i] = zero_acc();
    {
      int tid = G * 256 + cg * 64 + lane_id();
      if (tid < 2 * SQ) *(LDS3 float*)(uintptr_t)(L_M + tid * 4) = -__builtin_inff();
    }
    const char* pfb = (cg & 1) ? vb + (size_t)(cg >> 1) * UNIT : kb + (size_t)cg * UNIT;
    const unsigned pfo = (unsigned)lane_id() * 64u;
    auto pf = [&](int j, int k) {
      if (G == 1 && j < 3 * NPF) {
        const int r = (j * 11) >> 5;
        const int du = (cg == 0 && r > 0) ? 2 * r + 1 : 2 * r;
        warm(pfo, pfb + (size_t)(du * UNIT + (2 * (j - 3 * r) + k) * 4096));
      }
    };
    qphase(1, 2, 0, false, true, [&](int m) { if (G == 0 && m >= 3 && m < 8) x1(xb + 128, PBUF, m - 3); if (m == 9) pf(0, 0); });
    qphase(0, 2, 0, true, true, [&](int m) { if (G == 0 && (m & 1) && m < 10) x1(xb + 256, -L_PB, m >> 1); if (m == 9) pf(0, 1); });
    if (G == 0) WAIT_VM(5);
    BAR();
    int wu = 3;
#pragma unroll 1
    for (int j = 1; j <= 7; ++j) {
      const char* wn = wb + (size_t)(j + 2) * 64 * NH * DH * 2;
      const int un = wu == 2 ? 4 : wu - 1;
      const int xo = wu == 4 ? -L_PB : (wu - 2) * PBUF, xn = un == 4 ? -L_PB : (un - 2) * PBUF;
      qphase(1, wu, xo, true, true, [&](int m) { if (G == 0 && m < 10 && (m & 1)) x1(xb + (j + 2) * 128, xn, m >> 1); if (G == 0 && m == 11) unit1(wn, un, 0); if (m == 9) pf(j, 0); });
      qphase(0, wu, xo, true, true, [&](int m) { if (G == 0 && !(m & 1) && m < 10) unit1(wn, un, 1 + (m >> 1)); if (m == 9) pf(j, 1); });
      if (G == 0) WAIT_VM(11);
      BAR();
      wu = wu == 4 ? 2 : wu + 1;
    }
    qphase(1, 4, -L_PB, true, true, [&](int m) { if (m == 9) pf(8, 0); });
    qphase(0, 4, -L_PB, true, true, [&](int m) { if (m == 9) pf(8, 1); });
    if (G == 0) WAIT_VM(0); else mk_dofs();
    BAR();
    qphase(1, 2, 0, true, true, [&](int m) { if (G == 1 && (m & 1) && m < 12) unit1(kb, 0, m >> 1); });
    qphase(0, 2, 0, true, true, [&](int m) { if (G == 1 && (m & 1) && m < 12) unit1(kb + 2 * UNIT, 3, m >> 1); });
    BAR();
    if (G == 0) mk_kvo();
    qphase(1, 0, 0, true, false, [&](int m) { if (m % 3 == 1 && m < 9) kv1(vb, 2, m / 3); });
    const int lq = lane_id(), liq = lq & 15, gq = lq >> 4;
    const unsigned fkq = (unsigned)(liq >> 1);
#pragma unroll
    for (int t = 0; t < NP; ++t)
#pragma unroll
      for (int i = 0; i < 3; ++i) {
        unsigned ch = (unsigned)(2 * (3 * cg + i) + (gq >> 1));
        unsigned a = L_Q + (unsigned)((16 * (PT0 + t) + liq) * 384) + ((ch ^ fkq) << 4) + (unsigned)((gq & 1) * 8);
        *(LDS3 u32x2*)(uintptr_t)a = pack4(acc[t][i][0], acc[t][i][1], acc[t][i][2], acc[t][i][3]);
        acc[t][i] = zero_acc();
      }
    BAR();
    bf16x8 qf[NQ][6];
    {
      unsigned q0 = L_Q + (unsigned)((16 * QT0 + liq) * 384) + (((unsigned)gq ^ fkq) << 4);
#pragma unroll
      for (int t = 0; t < NQ; ++t)
#pragma unroll
        for (int ks = 0; ks < 6; ++ks) qf[t][ks] = lds_b128(((ks & 1) ? (q0 ^ 64u) : q0) + t * 6144 + (ks >> 1) * 128);
    }
    if (G == 1) WAIT_VM(9);
    BAR();
    issue_kv(vb + UNIT, 4);

    constexpr float THR = 8.0f;
    f32x4 s[NQ];
    const f32x4 zero4 = {0.f, 0.f, 0.f, 0.f};
    float mc[NM], lsum[NQ], alpha[NP];
    bf16x8 kf[3];
    int flg = 0;
    bool pend = false;
#pragma unroll
    for (int t = 0; t < NM; ++t) mc[t] = 0.f;
#pragma unroll
    for (int t = 0; t < NQ; ++t) lsum[t] = 0.f;
#pragma unroll
    for (int t = 0; t < NP; ++t) alpha[t] = 1.f;

    auto qk_pre = [&](int unit) {
      unsigned k0 = addv(kofs0, unit * UNIT);
      kf[0] = lds_b128(k0);
      kf[1] = lds_b128(k0 ^ 64u);
    };
    auto qk = [&](int unit, auto&& slot) {
      unsigned k0 = addv(kofs0, unit * UNIT), k1 = k0 ^ 64u;
#pragma unroll
      for (int ks = 0; ks < 6; ++ks) {
#pragma unroll
        for (int t = 0; t < NQ; ++t) {
          s[t] = MFMA(kf[ks % 3], qf[t][ks], ks == 0 ? zero4 : s[t]);
          SB();
          if (t == 0 && ks + 2 < 6) kf[(ks + 2) % 3] = lds_b128(((ks & 1) ? k1 : k0) + ((ks + 2) >> 1) * 128);
          if (t >= 1) slot(ks * 4 + t);
          SB();
        }
      }
    };
    auto check = [&](int par, int gen, bool first) {
      float mx = -__builtin_inff();
#pragma unroll
      for (int t = 0; t < NQ; ++t) {
#pragma unroll
        for (int e = 0; e < 4; ++e) s[t][e] = fmaf(s[t][e], cs, mc[QO + t]);
        mx = max3f(max3f(mx, s[t][0], s[t][1]), s[t][2], s[t][3]);
      }
      if (first || __builtin_amdgcn_ballot_w64(!(mx <= THR)) != 0) {
        const unsigned mb = addv(mofs, par * SQ * 4);
#pragma unroll
        for (int t = 0; t < NQ; ++t) {
          float y = fmaxf(fmaxf(s[t][0], s[t][1]), fmaxf(s[t][2], s[t][3])) - mc[QO + t];
          __hip_atomic_fetch_max((LDS3 float*)(uintptr_t)(mb + (unsigned)((QO + t) * 64)), y,
                                 __ATOMIC_RELAXED, __HIP_MEMORY_SCOPE_WORKGROUP);
        }
        *(volatile LDS3 int*)(uintptr_t)(L_F + par * 4) = gen;
      }
    };
    auto ld_flag = [&](int par) { flg = *(volatile LDS3 int*)(uintptr_t)(L_F + par * 4); };
    auto update = [&](int par, int gen, bool first) {
      if (first || __builtin_amdgcn_readfirstlane(flg) == gen) {
        const unsigned mb = addv(mofs, par * SQ * 4);
#pragma unroll
        for (int t = 0; t < NM; ++t) {
          float m = *(const LDS3 float*)(uintptr_t)(mb + (unsigned)(t * 64));
          float mn = first ? -m : fminf(mc[t], -m);
          float d = mn - mc[t];
          mc[t] = mn;
          float al = __builtin_amdgcn_exp2f(d);
          if (t >= QO && t < QO + NQ) {
#pragma unroll
            for (int e = 0; e < 4; ++e) s[t - QO][e] += d;
            if (!first) lsum[t - QO] *= al;
          }
          if (!first && t < NP) alpha[t] = al;
        }
        if (!first) pend = true;
      }
    };
    auto softmax = [&](int par, auto&& slot) {
      const unsigned pwb = addv(pw, par * PBUF);
#pragma unroll
      for (int t = 0; t < NQ; ++t) {
        float p0 = __builtin_amdgcn_exp2f(s[t][0]);
        float p1 = __builtin_amdgcn_exp2f(s[t][1]);
        SB();
        slot(t);
        SB();
        float p2 = __builtin_amdgcn_exp2f(s[t][2]);
        float p3 = __builtin_amdgcn_exp2f(s[t][3]);
        lsum[t] += (p0 + p1) + (p2 + p3);
        PIN(lsum[t]);
        *(LDS3 u32x2*)(uintptr_t)(pwb + (unsigned)(t * 2048)) = pack4(p0, p1, p2, p3);
        SB();
      }
    };
    auto rescale = [&]() {
      if (pend) {
        asm volatile("s_nop 7\n\ts_nop 7\n\ts_nop 3" ::: "memory");
#pragma unroll
        for (int t = 0; t < NP; ++t)
#pragma unroll
          for (int i = 0; i < 3; ++i) {
#pragma unroll
            for (int e = 0; e < 4; ++e) {
              float v = acc[t][i][e];
              asm volatile("v_mul_f32 %0, %0, %1" : "+v"(v) : "v"(alpha[t]));
              acc[t][i][e] = v;
            }
          }
        asm volatile("s_nop 1" ::: "memory");
        pend = false;
      }
    };
    auto ld_v = [&](int ks, int unit, int i) {
      unsigned va = addv(vofs[i], unit * UNIT) + ks * 12288;
      fa[ks][i] = lds_tr2(va, va + 1536);
    };
    auto rs = [&](int st, int sh) { return (st >= NP ? st + sh : st) & 3; };
    constexpr int SH = G == 0 ? 0 : 2;
    auto ld_p = [&](int par, int st, int sh = G == 0 ? 0 : 2) {
      unsigned pa = addv(pofs0, par * PBUF);
      fr[rs(st, sh)] = lds_b128((st >= NP ? pa ^ 64u : pa) + (st % NP) * 2048);
    };
    auto pv_steps = [&](int s0, int s1, int sh, auto&& slot) {
#pragma unroll
      for (int st = s0; st < s1; ++st)
#pragma unroll
        for (int i = 0; i < 3; ++i) {
          acc[st % NP][i] = MFMA(fa[st / NP][i], fr[rs(st, sh)], acc[st % NP][i]);
          SB();
          slot(st * 3 + i);
          SB();
        }
    };
    const int gen0 = it * 8 + 1;

    qk_pre(0);
    qk(0, noslot);
    check(0, gen0, true);
    BAR();
    qk_pre(1);
    update(0, gen0, true);
    SB();
    softmax(0, noslot);
    qk(1, noslot);
    check(1, gen0 + 1, false);
    WAIT_VM(3);
    BAR();
    int uv = 2;
    if (G == 1) {
#pragma unroll
      for (int i = 0; i < 3; ++i) fa[1][i] = __builtin_bit_cast(bf16x8, zero_acc());
#pragma unroll
      for (int i = 0; i < 4; ++i) fr[i] = __builtin_bit_cast(bf16x8, zero_acc());
    }
    const char* wnx = A.wq[1] + (size_t)h * DH * 2;
    auto interval = [&](int c, auto kind_c) {
      constexpr int KIND = decltype(kind_c)::value;
      constexpr bool LAST = KIND == 3;
      const int uk = uv == 4 ? 0 : uv + 1;
      const int u3 = uv >= 2 ? uv - 2 : uv + 3;
      const int u4 = uv >= 1 ? uv - 1 : uv + 4;
      const int pp = (c - 1) & 1;
      const char* src3 = KIND <= 1 ? kb + (size_t)(c + 2) * UNIT : vb + (size_t)7 * UNIT;
      const char* src4 = vb + (size_t)(c + 1) * UNIT;
      auto d3 = [&](int j) { if (KIND == 1 || KIND == 2) kv1(src3, u3, j); };
      auto d4 = [&](int j) { if (KIND <= 1) kv1(src4, u4, j); };
      if (G == 0) {
        ld_flag(c & 1);
        SB();
        ld_v(0, uv, 0); ld_p(pp, 0); ld_v(0, uv, 1); ld_v(0, uv, 2); ld_p(pp, 1);
        if (LAST) mk_dofs();
        SB();
        update(c & 1, gen0 + c, false);
        SB();
        softmax(c & 1, [&](int t) { if (LAST) { if (it == 0) unit1(wnx, 2, t); } else if (t < 3) d3(t); });
        pv_steps(0, 8, SH, [&](int m) {
          const int st = m / 3, i = m % 3;
          if (i == 0 && st + 2 < 8) ld_p(pp, st + 2);
          if (i == 1 && st < 3) ld_v(1, uv, st);
          if (!LAST && i == 1 && st >= 3 && st < 6) d4(st - 3);
          if (!LAST && m == 19) qk_pre(uk);
          if (LAST && it == 0 && i == 2 && st < 7) { if (st == 0) unit1(wnx, 2, 5); else unit1(wnx + (size_t)64 * NH * DH * 2, 3, st - 1); }
        });
        rescale();
        if (!LAST) {
          qk(uk, noslot);
          check((c + 1) & 1, gen0 + c + 1, false);
          WAIT_VM(3);
        }
      } else {
        ld_flag(c & 1);
        pv_steps(7, 10, SH, [&](int m) {
          if (m == 21) ld_v(0, uv, 0);
          if (m == 22) ld_p(pp, 0);
          if (m == 23) ld_v(0, uv, 1);
          if (m == 24) ld_v(0, uv, 2);
          if (m == 25) ld_p(pp, 1);
          if (m == 26) d3(0);
          if (m == 27) ld_p(pp, 2);
          if (m == 28) d3(1);
        });
        rescale();
        pv_steps(0, 5, SH, [&](int m) {
          if (m == 0) ld_p(pp, 3);
          if (m == 1) d3(2);
          if (m == 3) ld_p(pp, 4);
          if (!LAST && m == 10) qk_pre(uk);
        });
        update(c & 1, gen0 + c, false);
        SB();
        softmax(c & 1, [&](int t) { if (t < 3) d4(t); });
        if (KIND == 2 && it == 0) { mk_dofs(); issue_unit(A.k[1] + kvoff + UNIT, u4); }
        if (!LAST) {
          qk(uk, [&](int m) {
            if (m == 13) ld_v(1, uv, 0);
            if (m == 14) ld_p(pp, 5);
            if (m == 17) ld_v(1, uv, 1);
            if (m == 18) ld_v(1, uv, 2);
            if (m == 21) ld_p(pp, 6);
            if (m == 22) ld_p(pp, 7);
          });
          check((c + 1) & 1, gen0 + c + 1, false);
        } else {
          ld_v(1, uv, 0); ld_p(pp, 5); ld_v(1, uv, 1); ld_v(1, uv, 2); ld_p(pp, 6); ld_p(pp, 7);
        }
        SB();
        pv_steps(5, 7, SH, [&](int m) { if (m == 15) ld_p(pp, 8); if (m == 18) ld_p(pp, 9); });
        if (!LAST) { if (KIND == 2 && it == 0) WAIT_VM(9); else WAIT_VM(3); }
      }
      uv = uv >= 3 ? uv - 3 : uv + 2;
    };
#pragma unroll 1
    for (int c = 1; c <= 5; ++c) {
      interval(c, std::integral_constant<int, 1>{});
      BAR();
    }
    interval(6, std::integral_constant<int, 2>{});
    BAR();
    interval(7, std::integral_constant<int, 3>{});
    const int le = lane_id();
#pragma unroll
    for (int t = 0; t < NQ; ++t) {
      float l = lsum[t];
      l += __builtin_bit_cast(float, __builtin_amdgcn_ds_bpermute((le ^ 16) << 2, __builtin_bit_cast(int, l)));
      l += __builtin_bit_cast(float, __builtin_amdgcn_ds_bpermute((le ^ 32) << 2, __builtin_bit_cast(int, l)));
      if (le < 16) *(LDS3 float*)(uintptr_t)(L_L + (unsigned)(cg * SQ * 4 + (16 * (QT0 + t) + le) * 4)) = l;
    }
    if (G == 0) { if (it == 0) WAIT_VM(12); else WAIT_VM(0); }
    else { if (it == 0) WAIT_VM(6); else WAIT_VM(0); }
    BAR();
    if (G == 0) {
      ld_v(0, 0, 0); ld_p(1, 0); ld_v(0, 0, 1); ld_v(0, 0, 2); ld_p(1, 1);
      SB();
      if (it == 0) issue_x(A.x[1] + (size_t)b * SQ * DM * 2, 0);
      pv_steps(0, 8, SH, [&](int m) {
        const int st = m / 3, i = m % 3;
        if (i == 0 && st + 2 < 8) ld_p(1, st + 2);
        if (i == 1 && st < 3) ld_v(1, 0, st);
        if (it == 0 && i == 2 && st < 6) unit1(wnx + (size_t)128 * NH * DH * 2, 4, st);
      });
    } else {
      if (it == 0) warm_wq(wnx);
      pv_steps(7, 10, SH, [&](int m) {
        if (m == 21) ld_v(0, 0, 0);
        if (m == 22) ld_p(1, 0);
        if (m == 23) ld_v(0, 0, 1);
        if (m == 24) ld_v(0, 0, 2);
        if (m == 25) ld_p(1, 1);
        if (m == 27) ld_p(1, 2);
      });
      rescale();
      pv_steps(0, 10, 0, [&](int m) {
        const int st = m / 3, i = m % 3;
        if (i == 0 && st + 3 < 10) ld_p(1, st + 3, 0);
        if (i == 1 && st < 3) ld_v(1, 0, st);
      });
    }
    {
      char* ob = A.ows + (size_t)((it * NB + b) * NH + h) * OWS_ITEM + le * 16;
      const int ufull = (3 * cg + 1) >> 1;
      const int uhalf = (cg & 1) ? (3 * cg) >> 1 : (3 * cg + 2) >> 1;
#pragma unroll
      for (int t = 0; t < NP; ++t) {
        unsigned la = L_L + (unsigned)((16 * (PT0 + t) + (le & 15)) * 4);
        float l = (*(const LDS3 float*)(uintptr_t)la + *(const LDS3 float*)(uintptr_t)(la + SQ * 4)) +
                  (*(const LDS3 float*)(uintptr_t)(la + 2 * SQ * 4) + *(const LDS3 float*)(uintptr_t)(la + 3 * SQ * 4));
        float inv = 1.0f / l;
        u32x2 o[3];
#pragma unroll
        for (int i = 0; i < 3; ++i) o[i] = pack4(acc[t][i][0] * inv, acc[t][i][1] * inv, acc[t][i][2] * inv, acc[t][i][3] * inv);
        u32x4 full; u32x2 half;
        if (cg & 1) { full = (u32x4){o[1][0], o[1][1], o[2][0], o[2][1]}; half = o[0]; }
        else        { full = (u32x4){o[0][0], o[0][1], o[1][0], o[1][1]}; half = o[2]; }
        *(u32x4*)(ob + (ufull >> 1) * PBUF + (PT0 + t) * 2048 + (ufull & 1) * 1024) = full;
        *(u32x2*)(ob + (uhalf >> 1) * PBUF + (PT0 + t) * 2048 + (uhalf & 1) * 1024 + (cg & 1) * 8) = half;
      }
    }
    if (G == 0 && it == 0) WAIT_VM(14);
    BAR();
  }
}

NO_PK __global__ __launch_bounds__(512, 2) void k1_attn(K1Args A) {
  extern __shared__ __attribute__((aligned(16))) char shm[];
  const int w = __builtin_amdgcn_readfirstlane(threadIdx.x >> 6);
  const int xcd = blockIdx.x & 7, idx = blockIdx.x >> 3;
  const int h = xcd * 4 + (idx & 3), b = idx >> 2;
  if (w < 4) k1_body<0>(A, w, b, h);
  else k1_body<1>(A, w - 4, b, h);
}

constexpr int K2_STAGE = PBUF + 64 * 320;
constexpr int K2_NST = 4;
constexpr int K2_CH = 24;
struct K2Args { const char* wo[2]; const char* ows; float* part; };

template <int NR, int NC>
__device__ __forceinline__ void k2_body(const K2Args& A, int w, int lane, int rt0, int ct0, int br, int b, int nb, int sp) {
  STAMP_INIT();
  const int g = lane >> 4;
  unsigned dofs[5]; int dlds[5]; bool isw[5];
#pragma unroll
  for (int i = 0; i < 5; ++i) {
    int p = w + 8 * i; if (p >= 38) p -= 2;
    isw[i] = p >= 18;
    dlds[i] = p * 1024;
    if (p < 18) dofs[i] = (unsigned)(p * 1024 + lane * 16);
    else {
      int s = 64 * (p - 18) + lane, r = s / 20, c = s - r * 20;
      dofs[i] = (unsigned)(r * (DM * 2) + ((c ^ (((r >> 2) & 1) << 1)) << 4));
    }
  }
  const char* ob = A.ows + ((size_t)(br * NB + b) * NH * 3 + (size_t)sp * K2_CH) * PBUF;
  const char* wb = A.wo[br] + (size_t)sp * K2_CH * 64 * DM * 2 + nb * 320;
  auto issue1 = [&](int ci, int i) {
    dma16((unsigned)((ci & 3) * K2_STAGE) + (unsigned)dlds[i], dofs[i], isw[i] ? wb + (size_t)ci * 64 * DM * 2 : ob + (size_t)ci * PBUF);
  };
  auto issue = [&](int ci) {
#pragma unroll
    for (int i = 0; i < 5; ++i) issue1(ci, i);
  };
  unsigned aofs[NC];
  {
    const int q = (lane >> 2) & 3, p = lane & 3;
#pragma unroll
    for (int i = 0; i < NC; ++i) aofs[i] = PBUF + (unsigned)((4 * g + q) * 320 + (((ct0 + i) ^ (g & 1)) << 5) + p * 8);
  }
  const unsigned bofs = (unsigned)(rt0 * 2048 + lane * 16);
  f32x4 acc[NR][NC];
#pragma unroll
  for (int t = 0; t < NR; ++t)
#pragma unroll
    for (int i = 0; i < NC; ++i) acc[t][i] = (f32x4){0.f, 0.f, 0.f, 0.f};
  bf16x8 fa[2][NC], fb[2][NR];
  auto ld_one = [&](int ks, unsigned base, int r) {
    if (r < NC) fa[ks][r] = lds_tr2(base + aofs[r] + ks * 10240, base + aofs[r] + ks * 10240 + 5120);
    else if (r < NC + NR) fb[ks][r - NC] = lds_b128(base + bofs + (r - NC) * 2048 + ks * 1024);
  };
  auto phase = [&](int km, unsigned base, bool rd, int cd, int d0, int nd) {
#pragma unroll
    for (int m = 0; m < NR * NC; ++m) {
      acc[m / NC][m % NC] = MFMA(fa[km][m % NC], fb[km][m / NC], acc[m / NC][m % NC]);
      SB();
      if (rd) ld_one(1 - km, base, m);
      constexpr int GAP = NR * NC >= 12 ? 4 : 3;
      if (cd >= 0 && m % GAP == 1 && m / GAP < nd) issue1(cd, d0 + m / GAP);
      SB();
    }
  };
  issue(0); issue(1); issue(2);
  WAIT_VM(10);
  BAR();
#pragma unroll
  for (int r = 0; r < NC + NR; ++r) ld_one(0, 0u, r);
  SB();
  issue(3);
  SB();
  phase(0, 0u, true, -1, 0, 0);
  WAIT_VM(10);
  BAR();
  for (int ci = 1; ci < K2_CH - 3; ++ci) {
    unsigned base = (unsigned)((ci & 3) * K2_STAGE);
    phase(1, base, true, ci + 3, 0, 3);
    phase(0, base, true, ci + 3, 3, 2);
    WAIT_VM(10);
    BAR();
  }
#pragma unroll
  for (int ci = K2_CH - 3; ci < K2_CH; ++ci) {
    unsigned base = (unsigned)((ci & 3) * K2_STAGE);
    phase(1, base, true, -1, 0, 0);
    phase(0, base, true, -1, 0, 0);
    if (ci == K2_CH - 3) { WAIT_VM(5); BAR(); } else if (ci == K2_CH - 2) { WAIT_VM(0); BAR(); }
  }
  phase(1, 0u, false, -1, 0, 0);
  float* pb = A.part + ((size_t)(((br * NB + b) * 4 + nb) * 4 + sp) * 90) * 256 + lane * 4;
#pragma unroll
  for (int t = 0; t < NR; ++t)
#pragma unroll
    for (int i = 0; i < NC; ++i) *(f32x4*)(pb + ((rt0 + t) * 10 + ct0 + i) * 256) = acc[t][i];
}

__global__ __launch_bounds__(512, 2) void k2_oproj(K2Args A) {
  extern __shared__ __attribute__((aligned(16))) char shm[];
  const int w = __builtin_amdgcn_readfirstlane(threadIdx.x >> 6);
  const int lane = threadIdx.x & 63;
  const int xcd = blockIdx.x & 7, idx = blockIdx.x >> 3;
  const int br = xcd >> 2, sp = xcd & 3, b = idx >> 2, nb = idx & 3;
  const int cg = w & 3;
  if (w < 2) k2_body<5, 3>(A, w, lane, 0, 3 * cg, br, b, nb, sp);
  else if (w < 4) k2_body<5, 2>(A, w, lane, 0, 2 * cg + 2, br, b, nb, sp);
  else if (w < 6) k2_body<4, 2>(A, w, lane, 5, 2 * cg, br, b, nb, sp);
  else k2_body<4, 3>(A, w, lane, 5, 3 * cg - 2, br, b, nb, sp);
}

__global__ __launch_bounds__(256) void k3_reduce(const float* __restrict__ part, char* __restrict__ out) {
  int gid = blockIdx.x * 256 + threadIdx.x;
  int lane = gid & 63, f = (gid >> 6) % 90, tile = (gid >> 6) / 90;
  const float* p = part + ((size_t)tile * 4 * 90 + f) * 256 + lane * 4;
  f32x4 s = *(const f32x4*)p;
  s += *(const f32x4*)(p + 90 * 256);
  s += *(const f32x4*)(p + 2 * 90 * 256);
  s += *(const f32x4*)(p + 3 * 90 * 256);
  int nb = tile & 3, b = (tile >> 2) & 7, br = tile >> 5;
  int rt = f / 10, ct = f - rt * 10;
  int q = rt * 16 + (lane & 15), n = br * DM + nb * 160 + ct * 16 + (lane >> 4) * 4;
  *(u32x2*)(out + ((size_t)(b * SQ + q) * (2 * DM) + n) * 2) = pack4(s[0], s[1], s[2], s[3]);
}

extern "C" void kernel_launch(void* const* d_in, const int* in_sizes, int n_in, void* d_out, int out_size,
                              void* d_ws, size_t ws_size, hipStream_t stream) {
  (void)in_sizes; (void)n_in; (void)out_size; (void)ws_size;
  K1Args a1;
  for (int i = 0; i < 2; ++i) {
    a1.x[i] = (const char*)d_in[5 * i + 0];
    a1.k[i] = (const char*)d_in[5 * i + 1];
    a1.v[i] = (const char*)d_in[5 * i + 2];
    a1.wq[i] = (const char*)d_in[5 * i + 3];
  }
  a1.ows = (char*)d_ws;
  K2Args a2;
  a2.wo[0] = (const char*)d_in[4];
  a2.wo[1] = (const char*)d_in[9];
  a2.ows = (const char*)d_ws;
  a2.part = (float*)((char*)d_ws + OWS_BYTES);
  (void)hipFuncSetAttribute((const void*)k1_attn, hipFuncAttributeMaxDynamicSharedMemorySize, L_TOT1);
  (void)hipFuncSetAttribute((const void*)k2_oproj, hipFuncAttributeMaxDynamicSharedMemorySize, K2_NST * K2_STAGE);
  k1_attn<<<256, 512, L_TOT1, stream>>>(a1);
  k2_oproj<<<256, 512, K2_NST * K2_STAGE, stream>>>(a2);
  k3_reduce<<<64 * 90 * 64 / 256, 256, 0, stream>>>(a2.part, (char*)d_out);
}
```

```cpp
#include <hip/hip_runtime.h>
#include <hip/hip_bf16.h>
#include <stdint.h>
#include <type_traits>

typedef __attribute__((ext_vector_type(4))) short s16x4;
typedef __attribute__((ext_vector_type(8))) short s16x8;
typedef __attribute__((ext_vector_type(8))) __bf16 bf16x8;
typedef __attribute__((ext_vector_type(4))) __bf16 bf16x4;
typedef __attribute__((ext_vector_type(4))) float f32x4;
typedef __attribute__((ext_vector_type(2))) float f32x2;
typedef __attribute__((ext_vector_type(2))) unsigned u32x2;
typedef __attribute__((ext_vector_type(4))) unsigned u32x4;
#define LDS3 __attribute__((address_space(3)))

constexpr int NB = 8, SQ = 144, DM = 640, NH = 32, SK = 512, DH = 192;
constexpr int UNIT = 24576;
constexpr int PBUF = 18432;
constexpr int L_PB = 5 * UNIT;
constexpr int L_M = L_PB + 2 * PBUF;
constexpr int L_L = L_M + 2 * SQ * 4;
constexpr int L_F = L_L + 4 * SQ * 4;
constexpr int L_TOT1 = L_F + 16;
constexpr int L_Q = 4 * UNIT;
constexpr size_t OWS_ITEM = 55296;
constexpr size_t OWS_BYTES = 512 * OWS_ITEM;

#define WAIT_VM(n) asm volatile("s_waitcnt vmcnt(" #n ")" ::: "memory")
#define STAMP_INIT() do {} while (0)
#define STAMP() do {} while (0)
#define BAR() do { asm volatile("" ::: "memory"); __builtin_amdgcn_s_waitcnt(0xC07F); __builtin_amdgcn_s_barrier(); asm volatile("" ::: "memory"); STAMP(); } while (0)

__device__ __forceinline__ void dma16(unsigned lds, unsigned voff, const char* base) {
  asm volatile("s_mov_b32 m0, %0\n\ts_nop 0\n\tglobal_load_lds_dwordx4 %1, %2"
               :: "s"(lds), "v"(voff), "s"(base) : "memory");
}
__device__ __forceinline__ bf16x8 lds_b128(unsigned a) {
  return *(const LDS3 bf16x8*)(uintptr_t)a;
}
__device__ __forceinline__ bf16x8 lds_tr2(unsigned a0, unsigned a1) {
  s16x4 x = __builtin_amdgcn_ds_read_tr16_b64_v4i16((LDS3 s16x4*)(uintptr_t)a0);
  s16x4 y = __builtin_amdgcn_ds_read_tr16_b64_v4i16((LDS3 s16x4*)(uintptr_t)a1);
  s16x8 z = __builtin_shufflevector(x, y, 0, 1, 2, 3, 4, 5, 6, 7);
  return __builtin_bit_cast(bf16x8, z);
}
__device__ __forceinline__ u32x2 pack4(float a, float b, float c, float d) {
  bf16x4 p; p[0] = (__bf16)a; p[1] = (__bf16)b; p[2] = (__bf16)c; p[3] = (__bf16)d;
  return __builtin_bit_cast(u32x2, p);
}
__device__ __forceinline__ int lane_id() {
  int r; asm volatile("v_mbcnt_lo_u32_b32 %0, -1, 0\n\tv_mbcnt_hi_u32_b32 %0, -1, %0" : "=v"(r)); return r;
}
__device__ __forceinline__ unsigned addv(unsigned v, int sc) {
  unsigned r; asm volatile("v_add_u32 %0, %1, %2" : "=v"(r) : "s"(sc), "v"(v)); return r;
}
__device__ __forceinline__ void bump(unsigned& v, int sc) { asm volatile("v_add_u32 %0, %1, %0" : "+v"(v) : "s"(sc)); }
__device__ __forceinline__ unsigned x64(unsigned v) { unsigned r; asm volatile("v_xor_b32 %0, 64, %1" : "=v"(r) : "v"(v)); return r; }
__device__ __forceinline__ f32x4 zero_acc() {
  float z; asm volatile("v_mov_b32 %0, 0" : "=v"(z)); return (f32x4){z, z, z, z};
}
#if defined(__HIP_DEVICE_COMPILE__)
#define NO_PK __attribute__((target("no-packed-fp32-ops")))
#else
#define NO_PK
#endif
#define PIN(x) asm volatile("" : "+v"(x))
#define MFMA(a, b, c) __builtin_amdgcn_mfma_f32_16x16x32_bf16((a), (b), (c), 0, 0, 0)

struct K1Args {
  const char* x[2]; const char* k[2]; const char* v[2]; const char* wq[2];
  char* ows;
};

#define SB() __builtin_amdgcn_sched_barrier(0)
__device__ __forceinline__ void dma16x3(unsigned lds, unsigned v0, unsigned v1, unsigned v2, const char* base) {
  asm volatile("s_mov_b32 m0, %0\n\ts_nop 0\n\tglobal_load_lds_dwordx4 %1, %4\n\t"
               "global_load_lds_dwordx4 %2, %4 offset:1024\n\tglobal_load_lds_dwordx4 %3, %4 offset:2048"
               :: "s"(lds), "v"(v0), "v"(v1), "v"(v2), "s"(base) : "memory");
}

template <int OFF>
__device__ __forceinline__ void dma16o(unsigned lds, unsigned v, const char* base) {
  asm volatile("s_mov_b32 m0, %0\n\ts_nop 0\n\tglobal_load_lds_dwordx4 %1, %2 offset:%3"
               :: "s"(lds), "v"(v), "s"(base), "n"(OFF) : "memory");
}
__device__ __forceinline__ void dma16j(int j, unsigned lds, unsigned v, const char* base) {
  if (j == 0) dma16o<0>(lds, v, base); else if (j == 1) dma16o<1024>(lds, v, base); else dma16o<2048>(lds, v, base);
}
__device__ __forceinline__ float max3f(float a, float b, float c) { return fmaxf(fmaxf(a, b), c); }

template <int G>
__device__ __forceinline__ void k1_body(const K1Args& A, int cg, int b, int h) {
  const int lane = lane_id();
  STAMP_INIT();
  constexpr int NQ = G == 0 ? 5 : 4, QT0 = G == 0 ? 0 : 5;
  constexpr int NP = G == 0 ? 4 : 5, PT0 = G == 0 ? 0 : 4;
  constexpr int NM = 5;
  constexpr int MT0 = G == 0 ? 0 : 4;
  constexpr int QO = QT0 - MT0;
  const float cs = 0.07216878364870322f * 1.4426950408889634f;
  const int li = lane & 15, g = lane >> 4;
  const unsigned fv2 = (unsigned)((((li >> 1) & 1) | ((li >> 2) & 2)) << 1);
  const unsigned fk = (unsigned)(li >> 1);
  unsigned kofs0 = (unsigned)((16 * cg + li) * 384) + (((unsigned)g ^ fv2) << 4);
  unsigned pofs0 = L_PB + (unsigned)((16 * PT0 + li) * 128) + (((unsigned)g ^ fk) << 4);
  unsigned vofs[3];
  {
    const int q = (lane >> 2) & 3, p = lane & 3;
    const unsigned fv = (unsigned)(((q >> 1) & 1) | ((g & 1) << 1));
#pragma unroll
    for (int i = 0; i < 3; ++i) {
      unsigned t = (unsigned)(3 * cg + i);
      vofs[i] = (unsigned)((8 * g + q) * 384) + ((t ^ fv) << 5) + (unsigned)(p * 8);
    }
  }
  const unsigned pw = L_PB + (unsigned)((16 * QT0 + li) * 128) + ((((unsigned)(2 * cg + (g >> 1))) ^ fk) << 4) + (unsigned)((g & 1) * 8);
  const unsigned mofs = L_M + (unsigned)((16 * MT0 + li) * 4);
  unsigned dofs[6];
  unsigned kvo[3];
  unsigned xofs = 0;
  auto mk_dofs = [&]() {
    const int ln = lane_id();
#pragma unroll
    for (int j = 0; j < 6; ++j) {
      int s = 64 * j + ln, r = (s * 2731) >> 16, c = s - r * 24;
      int f = (((r >> 1) & 1) | ((r >> 2) & 2)) << 1;
      int ch = c ^ f;
      dofs[j] = (unsigned)((16 * cg + r) * (G == 0 ? NH * DH * 2 : 384) + ch * 16 - (j % 3) * 1024);
    }
    if (G == 0) xofs = (unsigned)((ln >> 3) * (DM * 2) + (((ln & 7) ^ ((4 * (cg & 1) + (ln >> 4)) & 7)) << 4));
  };
  auto mk_kvo = [&]() {
    const int ln = lane_id();
#pragma unroll
    for (int j = 0; j < 3; ++j) {
      int s = 64 * j + ln, r = (s * 2731) >> 16, c = s - r * 24;
      kvo[j] = (unsigned)((16 * cg + r) * 384 + (c ^ (((r >> 1) & 1) << 1)) * 16 - j * 1024);
    }
  };
  mk_dofs();

  const size_t kvoff = (size_t)(b * NH + h) * SK * DH * 2;
  auto issue_unit = [&](const char* src, int unit) {
    dma16x3((unsigned)(unit * UNIT + cg * 6144), dofs[0], dofs[1], dofs[2], src);
    dma16x3((unsigned)(unit * UNIT + cg * 6144 + 3072), dofs[3], dofs[4], dofs[5], src);
  };
  auto issue_kv = [&](const char* src, int unit) {
    if (G == 0) dma16x3((unsigned)(unit * UNIT + cg * 6144), kvo[0], kvo[1], kvo[2], src);
    else dma16x3((unsigned)(unit * UNIT + cg * 6144 + 3072), dofs[3], dofs[4], dofs[5], src);
  };
  auto unit1 = [&](const char* src, int unit, int j) {
    dma16j(j % 3, (unsigned)(unit * UNIT + cg * 6144 + (j / 3) * 3072), dofs[j], src);
  };
  auto kv1 = [&](const char* src, int unit, int j) {
    if (G == 0) dma16j(j, (unsigned)(unit * UNIT + cg * 6144), kvo[j], src);
    else dma16j(j, (unsigned)(unit * UNIT + cg * 6144 + 3072), dofs[3 + j], src);
  };
  auto x1 = [&](const char* src, int xo, int i) {
    int p = cg + 4 * i; if (p > 17) p -= 2;
    dma16((unsigned)(L_PB + xo + p * 1024), xofs, src + (size_t)p * 8 * DM * 2);
  };
  auto issue_x = [&](const char* src, int pb) {
#pragma unroll
    for (int i = 0; i < 5; ++i) {
      int p = cg + 4 * i; if (p > 17) p -= 2;
      dma16((unsigned)(L_PB + pb * PBUF + p * 1024), xofs, src + (size_t)p * 8 * DM * 2);
    }
  };

#define PRV 3
#define PRIO(v, p) do { if (PRV == (v)) __builtin_amdgcn_s_setprio(p); } while (0)
  if (G == 1) PRIO(0, 1);
  if (G == 0) {
    const char* xb = A.x[0] + (size_t)b * SQ * DM * 2;
    const char* wb = A.wq[0] + (size_t)h * DH * 2;
    issue_unit(wb, 2);
    issue_x(xb, 0);
    issue_unit(wb + (size_t)64 * NH * DH * 2, 3);
    issue_unit(wb + (size_t)128 * NH * DH * 2, 4);
    WAIT_VM(12);
  } else {
    issue_unit(A.k[0] + kvoff, 1);
    if (cg == 0 && lane < 2) *(LDS3 int*)(uintptr_t)(L_F + lane * 4) = 0;
  }
  BAR();

  int cv = 0, cp = 0, ck = 0;
  auto set_v = [&](int unit) { const int o = unit * UNIT; bump(vofs[0], o - cv); bump(vofs[1], o - cv); bump(vofs[2], o - cv); cv = o; };
  auto set_p = [&](int o) { bump(pofs0, o - cp); cp = o; };
  auto set_k = [&](int unit) { const int o = unit * UNIT; bump(kofs0, o - ck); ck = o; };
  f32x4 acc[NP][3];
  bf16x8 fa[2][3], fb[2][NP], fr[4];
  auto qphase = [&](int km, int unit, int xo, bool mm, bool rd, auto&& slot) {
    if (km == 1 && rd) { set_v(unit); set_p(xo); }
    const unsigned pa = km ? pofs0 : x64(pofs0);
#pragma unroll
    for (int m = 0; m < 3 * NP; ++m) {
      if (mm) acc[m / 3][m % 3] = MFMA(fa[km][m % 3], fb[km][m / 3], acc[m / 3][m % 3]);
      SB();
      if (rd) {
        if (m < 3) {
          const unsigned va = vofs[m] + (1 - km) * 12288;
          fa[1 - km][m] = lds_tr2(va, va + 1536);
        } else if (m < 3 + NP) fb[1 - km][m - 3] = lds_b128(pa + (m - 3) * 2048);
      }
      slot(m);
      SB();
    }
  };
  auto noslot = [&](int) {};

  for (int it = 0; it < 2; ++it) {
    const char* xb = A.x[it] + (size_t)b * SQ * DM * 2;
    const char* wb = A.wq[it] + (size_t)h * DH * 2;
    const char* kb = A.k[it] + kvoff;
    const char* vb = A.v[it] + kvoff;
#pragma unroll
    for (int t = 0; t < NP; ++t)
#pragma unroll
      for (int i = 0; i < 3; ++i) acc[t][i] = zero_acc();
    {
      int tid = G * 256 + cg * 64 + lane_id();
      if (tid < 2 * SQ) *(LDS3 float*)(uintptr_t)(L_M + tid * 4) = -__builtin_inff();
    }
    qphase(1, 2, 0, false, true, [&](int m) { if (G == 0 && m >= 3 && m < 8) x1(xb + 128, PBUF, m - 3); });
    qphase(0, 2, 0, true, true, [&](int m) { if (G == 0 && (m & 1) && m < 10) x1(xb + 256, -L_PB, m >> 1); });
    if (G == 0) WAIT_VM(5);
    BAR();
    int wu = 3;
#pragma unroll 1
    for (int j = 1; j <= 7; ++j) {
      const char* wn = wb + (size_t)(j + 2) * 64 * NH * DH * 2;
      const int un = wu == 2 ? 4 : wu - 1;
      const int xo = wu == 4 ? -L_PB : (wu - 2) * PBUF, xn = un == 4 ? -L_PB : (un - 2) * PBUF;
      qphase(1, wu, xo, true, true, [&](int m) { if (G == 0 && m < 10 && (m & 1)) x1(xb + (j + 2) * 128, xn, m >> 1); if (G == 0 && m == 11) unit1(wn, un, 0); });
      qphase(0, wu, xo, true, true, [&](int m) { if (G == 0 && !(m & 1) && m < 10) unit1(wn, un, 1 + (m >> 1)); });
      if (G == 0) WAIT_VM(11);
      BAR();
      wu = wu == 4 ? 2 : wu + 1;
    }
    qphase(1, 4, -L_PB, true, true, noslot);
    qphase(0, 4, -L_PB, true, true, noslot);
    if (G == 0) WAIT_VM(0); else mk_dofs();
    BAR();
    qphase(1, 2, 0, true, true, [&](int m) { if (G == 1 && (m & 1) && m < 12) unit1(kb + UNIT, 0, m >> 1); });
    qphase(0, 2, 0, true, true, [&](int m) { if (G == 1 && (m & 1) && m < 12) unit1(kb + 2 * UNIT, 3, m >> 1); });
    BAR();
    if (G == 0) mk_kvo();
    qphase(1, 0, 0, true, false, [&](int m) { if (m % 3 == 1 && m < 9) kv1(vb, 2, m / 3); });
    const int lq = lane_id(), liq = lq & 15, gq = lq >> 4;
    const unsigned fkq = (unsigned)(liq >> 1);
#pragma unroll
    for (int t = 0; t < NP; ++t)
#pragma unroll
      for (int i = 0; i < 3; ++i) {
        unsigned ch = (unsigned)(2 * (3 * cg + i) + (gq >> 1));
        unsigned a = L_Q + (unsigned)((16 * (PT0 + t) + liq) * 384) + ((ch ^ fkq) << 4) + (unsigned)((gq & 1) * 8);
        *(LDS3 u32x2*)(uintptr_t)a = pack4(acc[t][i][0] * cs, acc[t][i][1] * cs, acc[t][i][2] * cs, acc[t][i][3] * cs);
        acc[t][i] = zero_acc();
      }
    BAR();
    bf16x8 qf[NQ][6];
    {
      unsigned q0 = L_Q + (unsigned)((16 * QT0 + liq) * 384) + (((unsigned)gq ^ fkq) << 4);
#pragma unroll
      for (int t = 0; t < NQ; ++t)
#pragma unroll
        for (int ks = 0; ks < 6; ++ks) qf[t][ks] = lds_b128(((ks & 1) ? (q0 ^ 64u) : q0) + t * 6144 + (ks >> 1) * 128);
    }
    if (G == 1) WAIT_VM(15);
    BAR();
    issue_kv(vb + UNIT, 4);

    constexpr float THR = 32.0f, ZR = 24.0f;
    bool nz = false;
    unsigned k1a = 0, p1a = 0;
    f32x4 s[NQ];
    const f32x4 zero4 = {0.f, 0.f, 0.f, 0.f};
    float mc[NM], lsum[NQ], alpha[NP];
    bf16x8 kf[3];
    int flg = 0;
    bool pend = false;
#pragma unroll
    for (int t = 0; t < NM; ++t) mc[t] = 0.f;
#pragma unroll
    for (int t = 0; t < NQ; ++t) lsum[t] = 0.f;
#pragma unroll
    for (int t = 0; t < NP; ++t) alpha[t] = 1.f;

    auto qk_pre = [&](int unit) {
      set_k(unit);
      k1a = x64(kofs0);
      kf[0] = lds_b128(kofs0);
      kf[1] = lds_b128(k1a);
    };
    auto qk = [&](int unit, auto&& slot) {
#pragma unroll
      for (int ks = 0; ks < 6; ++ks) {
#pragma unroll
        for (int t = 0; t < NQ; ++t) {
          s[t] = MFMA(kf[ks % 3], qf[t][ks], ks == 0 ? zero4 : s[t]);
          SB();
          if (t == 0 && ks + 2 < 6) kf[(ks + 2) % 3] = lds_b128(((ks & 1) ? k1a : kofs0) + ((ks + 2) >> 1) * 128);
          if (t >= 1) slot(ks * 4 + t);
          SB();
        }
      }
    };
    auto check = [&](int par, int gen, bool first) {
      if (nz) {
#pragma unroll
        for (int t = 0; t < NQ; ++t)
#pragma unroll
          for (int e = 0; e < 4; ++e) s[t][e] += mc[QO + t];
      }
      float mx = fmaxf(s[0][0], s[0][1]);
#pragma unroll
      for (int t = 0; t < NQ; ++t) mx = t ? max3f(max3f(mx, s[t][0], s[t][1]), s[t][2], s[t][3]) : max3f(mx, s[0][2], s[0][3]);
      if (first || __builtin_amdgcn_ballot_w64(!(mx <= THR)) != 0) {
        const unsigned mb = addv(mofs, par * SQ * 4);
#pragma unroll
        for (int t = 0; t < NQ; ++t) {
          float y = fmaxf(fmaxf(s[t][0], s[t][1]), fmaxf(s[t][2], s[t][3])) - mc[QO + t];
          __hip_atomic_fetch_max((LDS3 float*)(uintptr_t)(mb + (unsigned)((QO + t) * 64)), y,
                                 __ATOMIC_RELAXED, __HIP_MEMORY_SCOPE_WORKGROUP);
        }
        *(volatile LDS3 int*)(uintptr_t)(L_F + par * 4) = gen;
      }
    };
    auto ld_flag = [&](int par) { flg = *(volatile LDS3 int*)(uintptr_t)(L_F + par * 4); };
    auto update = [&](int par, int gen, bool first) {
      if (first || __builtin_amdgcn_readfirstlane(flg) == gen) {
        const unsigned mb = addv(mofs, par * SQ * 4);
#pragma unroll
        for (int t = 0; t < NM; ++t) {
          float m = *(const LDS3 float*)(uintptr_t)(mb + (unsigned)(t * 64));
          float mn = first ? (fabsf(m) <= ZR ? 0.f : -m) : fminf(mc[t], -m);
          float d = mn - mc[t];
          mc[t] = mn;
          float al = __builtin_amdgcn_exp2f(d);
          if (t >= QO && t < QO + NQ) {
#pragma unroll
            for (int e = 0; e < 4; ++e) s[t - QO][e] += d;
            if (!first) lsum[t - QO] *= al;
          }
          if (!first && t < NP) alpha[t] = al;
        }
        if (!first) { pend = true; nz = true; }
        else {
          bool z = true;
#pragma unroll
          for (int t = 0; t < NQ; ++t) z = z && mc[QO + t] == 0.f;
          nz = __builtin_amdgcn_ballot_w64(!z) != 0;
        }
      }
    };
    auto softmax = [&](int par, auto&& slot) {
      const unsigned pwb = addv(pw, par * PBUF);
#pragma unroll
      for (int t = 0; t < NQ; ++t) {
        float p0 = __builtin_amdgcn_exp2f(s[t][0]);
        float p1 = __builtin_amdgcn_exp2f(s[t][1]);
        SB();
        slot(t);
        SB();
        float p2 = __builtin_amdgcn_exp2f(s[t][2]);
        float p3 = __builtin_amdgcn_exp2f(s[t][3]);
        lsum[t] += (p0 + p1) + (p2 + p3);
        PIN(lsum[t]);
        *(LDS3 u32x2*)(uintptr_t)(pwb + (unsigned)(t * 2048)) = pack4(p0, p1, p2, p3);
        SB();
      }
    };
    auto rescale = [&]() {
      if (pend) {
        asm volatile("s_nop 7\n\ts_nop 7\n\ts_nop 3" ::: "memory");
#pragma unroll
        for (int t = 0; t < NP; ++t)
#pragma unroll
          for (int i = 0; i < 3; ++i) {
#pragma unroll
            for (int e = 0; e < 4; ++e) {
              float v = acc[t][i][e];
              asm volatile("v_mul_f32 %0, %0, %1" : "+v"(v) : "v"(alpha[t]));
              acc[t][i][e] = v;
            }
          }
        asm volatile("s_nop 1" ::: "memory");
        pend = false;
      }
    };
    auto ld_v = [&](int ks, int unit, int i) {
      if (ks == 0 && i == 0) set_v(unit);
      const unsigned va = vofs[i] + ks * 12288;
      fa[ks][i] = lds_tr2(va, va + 1536);
    };
    auto rs = [&](int st, int sh) { return (st >= NP ? st + sh : st) & 3; };
    constexpr int SH = G == 0 ? 0 : 2;
    auto ld_p = [&](int par, int st, int sh = G == 0 ? 0 : 2) {
      if (st == 0) set_p(par * PBUF);
      if (st == NP) p1a = x64(pofs0);
      fr[rs(st, sh)] = lds_b128((st >= NP ? p1a : pofs0) + (st % NP) * 2048);
    };
    auto pv_steps = [&](int s0, int s1, int sh, auto&& slot) {
#pragma unroll
      for (int st = s0; st < s1; ++st)
#pragma unroll
        for (int i = 0; i < 3; ++i) {
          acc[st % NP][i] = MFMA(fa[st / NP][i], fr[rs(st, sh)], acc[st % NP][i]);
          SB();
          slot(st * 3 + i);
          SB();
        }
    };
    const int gen0 = it * 8 + 1;

    qk_pre(1);
    qk(1, noslot);
    check(0, gen0, true);
    if (G == 1) WAIT_VM(12);
    BAR();
    qk_pre(0);
    update(0, gen0, true);
    SB();
    softmax(0, noslot);
    qk(0, noslot);
    check(1, gen0 + 1, false);
    WAIT_VM(3);
    BAR();
    int uv = 2;
    {
#pragma unroll
      for (int i = 0; i < 3; ++i) fa[1][i] = __builtin_bit_cast(bf16x8, zero_acc());
#pragma unroll
      for (int i = 0; i < 4; ++i) fr[i] = __builtin_bit_cast(bf16x8, zero_acc());
    }
    const char* wnx = A.wq[1] + (size_t)h * DH * 2;
    auto interval = [&](int c, auto kind_c) {
      constexpr int KIND = decltype(kind_c)::value;
      constexpr bool LAST = KIND == 3;
      const int uk = uv == 4 ? 0 : uv + 1;
      const int u3 = uv >= 2 ? uv - 2 : uv + 3;
      const int u4 = uv >= 1 ? uv - 1 : uv + 4;
      const int pp = (c - 1) & 1;
      const char* src3 = KIND <= 1 ? kb + (size_t)(c + 2) * UNIT : vb + (size_t)7 * UNIT;
      const char* src4 = vb + (size_t)(c + 1) * UNIT;
      auto d3 = [&](int j) { if (KIND == 1 || KIND == 2) kv1(src3, u3, j); };
      auto d4 = [&](int j) { if (KIND <= 1) kv1(src4, u4, j); };
      if (G == 0) {
        ld_flag(c & 1);
        pv_steps(6, 8, SH, [&](int m) {
          if (m == 18) ld_v(0, uv, 0);
          if (m == 19) ld_p(pp, 0);
          if (m == 20) ld_v(0, uv, 1);
          if (m == 21) ld_v(0, uv, 2);
          if (m == 22) ld_p(pp, 1);
        });
        if (LAST) mk_dofs();
        rescale();
        update(c & 1, gen0 + c, false);
        SB();
        PRIO(2, 1); PRIO(3, 1);
        softmax(c & 1, [&](int t) { if (LAST) { if (it == 0) unit1(wnx, 2, t); } else if (t < 3) d3(t); });
        PRIO(2, 0); PRIO(3, 0);
        pv_steps(0, LAST ? 6 : 5, SH, [&](int m) {
          const int st = m / 3, i = m % 3;
          if (i == 0 && st + 2 < (LAST ? 8 : 6)) ld_p(pp, st + 2);
          if (i == 1 && st < 3) ld_v(1, uv, st);
          if (i == 2 && st < 3) d4(st);
          if (!LAST && m == 10) qk_pre(uk);
          if (LAST && it == 0) {
            if (m == 2) unit1(wnx, 2, 5);
            if (m >= 5 && m < 17 && (m & 1)) unit1(wnx + (size_t)64 * NH * DH * 2, 3, (m - 5) >> 1);
          }
        });
        if (!LAST) {
          qk(uk, [&](int m) { if (m == 18) ld_p(pp, 6); if (m == 22) ld_p(pp, 7); });
          pv_steps(5, 6, SH, noslot);
          check((c + 1) & 1, gen0 + c + 1, false);
          WAIT_VM(3);
        }
      } else {
        PRIO(1, 1);
        ld_flag(c & 1);
        pv_steps(7, 10, SH, [&](int m) {
          if (m == 21) ld_v(0, uv, 0);
          if (m == 22) ld_p(pp, 0);
          if (m == 23) ld_v(0, uv, 1);
          if (m == 24) ld_v(0, uv, 2);
          if (m == 25) ld_p(pp, 1);
          if (m == 26) d3(0);
          if (m == 27) ld_p(pp, 2);
          if (m == 28) d3(1);
        });
        rescale();
        pv_steps(0, 5, SH, [&](int m) {
          if (m == 0) ld_p(pp, 3);
          if (m == 1) d3(2);
          if (m == 3) ld_p(pp, 4);
          if (!LAST && m == 10) qk_pre(uk);
        });
        update(c & 1, gen0 + c, false);
        SB();
        PRIO(2, 1); PRIO(3, 1);
        softmax(c & 1, [&](int t) { if (t < 3) d4(t); });
        PRIO(1, 0); PRIO(2, 0);
        if (KIND == 2 && it == 0) { mk_dofs(); issue_unit(A.k[1] + kvoff, u4); }
        if (!LAST) {
          qk(uk, [&](int m) {
            if (m == 13) ld_v(1, uv, 0);
            if (m == 14) ld_p(pp, 5);
            if (m == 17) ld_v(1, uv, 1);
            if (m == 18) ld_v(1, uv, 2);
            if (m == 21) ld_p(pp, 6);
            if (m == 22) ld_p(pp, 7);
          });
          pv_steps(5, 6, SH, [&](int m) { if (m == 15) ld_p(pp, 8); });
          check((c + 1) & 1, gen0 + c + 1, false);
        } else {
          ld_v(1, uv, 0); ld_p(pp, 5); ld_v(1, uv, 1); ld_v(1, uv, 2); ld_p(pp, 6); ld_p(pp, 7);
          pv_steps(5, 6, SH, [&](int m) { if (m == 15) ld_p(pp, 8); });
        }
        SB();
        PRIO(3, 0);
        pv_steps(6, 7, SH, [&](int m) { if (m == 18) ld_p(pp, 9); });
        if (!LAST) { if (KIND == 2 && it == 0) WAIT_VM(9); else WAIT_VM(3); }
      }
      uv = uv >= 3 ? uv - 3 : uv + 2;
    };
#pragma unroll 1
    for (int c = 1; c <= 5; ++c) {
      interval(c, std::integral_constant<int, 1>{});
      BAR();
    }
    interval(6, std::integral_constant<int, 2>{});
    BAR();
    interval(7, std::integral_constant<int, 3>{});
    const int le = lane_id();
#pragma unroll
    for (int t = 0; t < NQ; ++t) {
      float l = lsum[t];
      l += __builtin_bit_cast(float, __builtin_amdgcn_ds_bpermute((le ^ 16) << 2, __builtin_bit_cast(int, l)));
      l += __builtin_bit_cast(float, __builtin_amdgcn_ds_bpermute((le ^ 32) << 2, __builtin_bit_cast(int, l)));
      if (le < 16) *(LDS3 float*)(uintptr_t)(L_L + (unsigned)(cg * SQ * 4 + (16 * (QT0 + t) + le) * 4)) = l;
    }
    if (G == 0) { if (it == 0) WAIT_VM(12); else WAIT_VM(0); }
    else { if (it == 0) WAIT_VM(6); else WAIT_VM(0); }
    BAR();
    if (G == 0) {
      if (it == 0) issue_x(A.x[1] + (size_t)b * SQ * DM * 2, 0);
      pv_steps(6, 8, SH, [&](int m) {
        if (m == 18) ld_v(0, 0, 0);
        if (m == 19) ld_p(1, 0);
        if (m == 20) ld_v(0, 0, 1);
        if (m == 21) ld_v(0, 0, 2);
        if (m == 22) ld_p(1, 1);
      });
      rescale();
      pv_steps(0, 8, SH, [&](int m) {
        const int st = m / 3, i = m % 3;
        if (i == 0 && st + 2 < 8) ld_p(1, st + 2);
        if (i == 1 && st < 3) ld_v(1, 0, st);
        if (it == 0 && i == 2 && st < 6) unit1(wnx + (size_t)128 * NH * DH * 2, 4, st);
      });
    } else {
      pv_steps(7, 10, SH, [&](int m) {
        if (m == 21) ld_v(0, 0, 0);
        if (m == 22) ld_p(1, 0);
        if (m == 23) ld_v(0, 0, 1);
        if (m == 24) ld_v(0, 0, 2);
        if (m == 25) ld_p(1, 1);
        if (m == 27) ld_p(1, 2);
      });
      rescale();
      pv_steps(0, 10, 0, [&](int m) {
        const int st = m / 3, i = m % 3;
        if (i == 0 && st + 3 < 10) ld_p(1, st + 3, 0);
        if (i == 1 && st < 3) ld_v(1, 0, st);
      });
    }
    {
      char* ob = A.ows + (size_t)((it * NB + b) * NH + h) * OWS_ITEM + le * 16;
      const int ufull = (3 * cg + 1) >> 1;
      const int uhalf = (cg & 1) ? (3 * cg) >> 1 : (3 * cg + 2) >> 1;
#pragma unroll
      for (int t = 0; t < NP; ++t) {
        unsigned la = L_L + (unsigned)((16 * (PT0 + t) + (le & 15)) * 4);
        float l = (*(const LDS3 float*)(uintptr_t)la + *(const LDS3 float*)(uintptr_t)(la + SQ * 4)) +
                  (*(const LDS3 float*)(uintptr_t)(la + 2 * SQ * 4) + *(const LDS3 float*)(uintptr_t)(la + 3 * SQ * 4));
        float inv = 1.0f / l;
        u32x2 o[3];
#pragma unroll
        for (int i = 0; i < 3; ++i) o[i] = pack4(acc[t][i][0] * inv, acc[t][i][1] * inv, acc[t][i][2] * inv, acc[t][i][3] * inv);
        u32x4 full; u32x2 half;
        if (cg & 1) { full = (u32x4){o[1][0], o[1][1], o[2][0], o[2][1]}; half = o[0]; }
        else        { full = (u32x4){o[0][0], o[0][1], o[1][0], o[1][1]}; half = o[2]; }
        *(u32x4*)(ob + (ufull >> 1) * PBUF + (PT0 + t) * 2048 + (ufull & 1) * 1024) = full;
        *(u32x2*)(ob + (uhalf >> 1) * PBUF + (PT0 + t) * 2048 + (uhalf & 1) * 1024 + (cg & 1) * 8) = half;
      }
    }
    if (G == 0 && it == 0) WAIT_VM(14);
    BAR();
  }
}

NO_PK __global__ __launch_bounds__(512, 2) void k1_attn(K1Args A) {
  extern __shared__ __attribute__((aligned(16))) char shm[];
  const int w = __builtin_amdgcn_readfirstlane(threadIdx.x >> 6);
  const int xcd = blockIdx.x & 7, idx = blockIdx.x >> 3;
  const int h = xcd * 4 + (idx & 3), b = idx >> 2;
  if (w < 4) k1_body<0>(A, w, b, h);
  else k1_body<1>(A, w - 4, b, h);
}

constexpr int K2_STAGE = PBUF + 64 * 320;
constexpr int K2_NST = 4;
constexpr int K2_CH = 24;
struct K2Args { const char* wo[2]; const char* ows; float* part; };

template <int NR, int NC>
__device__ __forceinline__ void k2_body(const K2Args& A, int w, int lane, int rt0, int ct0, int br, int b, int nb, int sp) {
  STAMP_INIT();
  const int g = lane >> 4;
  unsigned dofs[5]; int dlds[5]; bool isw[5];
#pragma unroll
  for (int i = 0; i < 5; ++i) {
    int p = w + 8 * i; if (p >= 38) p -= 2;
    isw[i] = p >= 18;
    dlds[i] = p * 1024;
    if (p < 18) dofs[i] = (unsigned)(p * 1024 + lane * 16);
    else {
      int s = 64 * (p - 18) + lane, r = s / 20, c = s - r * 20;
      dofs[i] = (unsigned)(r * (DM * 2) + ((c ^ (((r >> 2) & 1) << 1)) << 4));
    }
  }
  const char* ob = A.ows + ((size_t)(br * NB + b) * NH * 3 + (size_t)sp * K2_CH) * PBUF;
  const char* wb = A.wo[br] + (size_t)sp * K2_CH * 64 * DM * 2 + nb * 320;
  auto issue1 = [&](int ci, int i) {
    dma16((unsigned)((ci & 3) * K2_STAGE) + (unsigned)dlds[i], dofs[i], isw[i] ? wb + (size_t)ci * 64 * DM * 2 : ob + (size_t)ci * PBUF);
  };
  auto issue = [&](int ci) {
#pragma unroll
    for (int i = 0; i < 5; ++i) issue1(ci, i);
  };
  unsigned aofs[NC];
  {
    const int q = (lane >> 2) & 3, p = lane & 3;
#pragma unroll
    for (int i = 0; i < NC; ++i) aofs[i] = PBUF + (unsigned)((4 * g + q) * 320 + (((ct0 + i) ^ (g & 1)) << 5) + p * 8);
  }
  const unsigned bofs = (unsigned)(rt0 * 2048 + lane * 16);
  f32x4 acc[NR][NC];
#pragma unroll
  for (int t = 0; t < NR; ++t)
#pragma unroll
    for (int i = 0; i < NC; ++i) acc[t][i] = (f32x4){0.f, 0.f, 0.f, 0.f};
  bf16x8 fa[2][NC], fb[2][NR];
  auto ld_one = [&](int ks, unsigned base, int r) {
    if (r < NC) fa[ks][r] = lds_tr2(base + aofs[r] + ks * 10240, base + aofs[r] + ks * 10240 + 5120);
    else if (r < NC + NR) fb[ks][r - NC] = lds_b128(base + bofs + (r - NC) * 2048 + ks * 1024);
  };
  auto phase = [&](int km, unsigned base, bool rd, int cd, int d0, int nd) {
#pragma unroll
    for (int m = 0; m < NR * NC; ++m) {
      acc[m / NC][m % NC] = MFMA(fa[km][m % NC], fb[km][m / NC], acc[m / NC][m % NC]);
      SB();
      if (rd) ld_one(1 - km, base, m);
      constexpr int GAP = NR * NC >= 12 ? 4 : 3;
      if (cd >= 0 && m % GAP == 1 && m / GAP < nd) issue1(cd, d0 + m / GAP);
      SB();
    }
  };
  issue(0); issue(1); issue(2);
  WAIT_VM(10);
  BAR();
#pragma unroll
  for (int r = 0; r < NC + NR; ++r) ld_one(0, 0u, r);
  SB();
  issue(3);
  SB();
  phase(0, 0u, true, -1, 0, 0);
  WAIT_VM(10);
  BAR();
  for (int ci = 1; ci < K2_CH - 3; ++ci) {
    unsigned base = (unsigned)((ci & 3) * K2_STAGE);
    phase(1, base, true, ci + 3, 0, 3);
    phase(0, base, true, ci + 3, 3, 2);
    WAIT_VM(10);
    BAR();
  }
#pragma unroll
  for (int ci = K2_CH - 3; ci < K2_CH; ++ci) {
    unsigned base = (unsigned)((ci & 3) * K2_STAGE);
    phase(1, base, true, -1, 0, 0);
    phase(0, base, true, -1, 0, 0);
    if (ci == K2_CH - 3) { WAIT_VM(5); BAR(); } else if (ci == K2_CH - 2) { WAIT_VM(0); BAR(); }
  }
  phase(1, 0u, false, -1, 0, 0);
  float* pb = A.part + ((size_t)(((br * NB + b) * 4 + nb) * 4 + sp) * 90) * 256 + lane * 4;
#pragma unroll
  for (int t = 0; t < NR; ++t)
#pragma unroll
    for (int i = 0; i < NC; ++i) *(f32x4*)(pb + ((rt0 + t) * 10 + ct0 + i) * 256) = acc[t][i];
}

__global__ __launch_bounds__(512, 2) void k2_oproj(K2Args A) {
  extern __shared__ __attribute__((aligned(16))) char shm[];
  const int w = __builtin_amdgcn_readfirstlane(threadIdx.x >> 6);
  const int lane = threadIdx.x & 63;
  const int xcd = blockIdx.x & 7, idx = blockIdx.x >> 3;
  const int br = xcd >> 2, sp = xcd & 3, b = idx >> 2, nb = idx & 3;
  const int cg = w & 3;
  if (w < 2) k2_body<5, 3>(A, w, lane, 0, 3 * cg, br, b, nb, sp);
  else if (w < 4) k2_body<5, 2>(A, w, lane, 0, 2 * cg + 2, br, b, nb, sp);
  else if (w < 6) k2_body<4, 2>(A, w, lane, 5, 2 * cg, br, b, nb, sp);
  else k2_body<4, 3>(A, w, lane, 5, 3 * cg - 2, br, b, nb, sp);
}

__global__ __launch_bounds__(256) void k3_reduce(const float* __restrict__ part, char* __restrict__ out) {
  int gid = blockIdx.x * 256 + threadIdx.x;
  int lane = gid & 63, f = (gid >> 6) % 90, tile = (gid >> 6) / 90;
  const float* p = part + ((size_t)tile * 4 * 90 + f) * 256 + lane * 4;
  f32x4 s = *(const f32x4*)p;
  s += *(const f32x4*)(p + 90 * 256);
  s += *(const f32x4*)(p + 2 * 90 * 256);
  s += *(const f32x4*)(p + 3 * 90 * 256);
  int nb = tile & 3, b = (tile >> 2) & 7, br = tile >> 5;
  int rt = f / 10, ct = f - rt * 10;
  int q = rt * 16 + (lane & 15), n = br * DM + nb * 160 + ct * 16 + (lane >> 4) * 4;
  *(u32x2*)(out + ((size_t)(b * SQ + q) * (2 * DM) + n) * 2) = pack4(s[0], s[1], s[2], s[3]);
}

extern "C" void kernel_launch(void* const* d_in, const int* in_sizes, int n_in, void* d_out, int out_size,
                              void* d_ws, size_t ws_size, hipStream_t stream) {
  (void)in_sizes; (void)n_in; (void)out_size; (void)ws_size;
  K1Args a1;
  for (int i = 0; i < 2; ++i) {
    a1.x[i] = (const char*)d_in[5 * i + 0];
    a1.k[i] = (const char*)d_in[5 * i + 1];
    a1.v[i] = (const char*)d_in[5 * i + 2];
    a1.wq[i] = (const char*)d_in[5 * i + 3];
  }
  a1.ows = (char*)d_ws;
  K2Args a2;
  a2.wo[0] = (const char*)d_in[4];
  a2.wo[1] = (const char*)d_in[9];
  a2.ows = (const char*)d_ws;
  a2.part = (float*)((char*)d_ws + OWS_BYTES);
  (void)hipFuncSetAttribute((const void*)k1_attn, hipFuncAttributeMaxDynamicSharedMemorySize, L_TOT1);
  (void)hipFuncSetAttribute((const void*)k2_oproj, hipFuncAttributeMaxDynamicSharedMemorySize, K2_NST * K2_STAGE);
  k1_attn<<<256, 512, L_TOT1, stream>>>(a1);
  k2_oproj<<<256, 512, K2_NST * K2_STAGE, stream>>>(a2);
  k3_reduce<<<64 * 90 * 64 / 256, 256, 0, stream>>>(a2.part, (char*)d_out);
}
```

```cpp
#include <hip/hip_runtime.h>
#include <hip/hip_bf16.h>
#include <stdint.h>
#include <type_traits>

typedef __attribute__((ext_vector_type(4))) short s16x4;
typedef __attribute__((ext_vector_type(8))) short s16x8;
typedef __attribute__((ext_vector_type(8))) __bf16 bf16x8;
typedef __attribute__((ext_vector_type(4))) __bf16 bf16x4;
typedef __attribute__((ext_vector_type(4))) float f32x4;
typedef __attribute__((ext_vector_type(2))) float f32x2;
typedef __attribute__((ext_vector_type(2))) unsigned u32x2;
typedef __attribute__((ext_vector_type(4))) unsigned u32x4;
#define LDS3 __attribute__((address_space(3)))

constexpr int NB = 8, SQ = 144, DM = 640, NH = 32, SK = 512, DH = 192;
constexpr int UNIT = 24576;
constexpr int PBUF = 18432;
constexpr int L_PB = 5 * UNIT;
constexpr int L_M = L_PB + 2 * PBUF;
constexpr int L_L = L_M + 2 * SQ * 4;
constexpr int L_F = L_L + 4 * SQ * 4;
constexpr int L_TOT1 = L_F + 16;
constexpr int L_Q = 4 * UNIT;
constexpr size_t OWS_ITEM = 55296;
constexpr size_t OWS_BYTES = 512 * OWS_ITEM;

#define WAIT_VM(n) asm volatile("s_waitcnt vmcnt(" #n ")" ::: "memory")
#define STAMP_INIT() do {} while (0)
#define STAMP() do {} while (0)
#define BAR() do { asm volatile("" ::: "memory"); __builtin_amdgcn_s_waitcnt(0xC07F); __builtin_amdgcn_s_barrier(); asm volatile("" ::: "memory"); STAMP(); } while (0)

__device__ __forceinline__ void dma16(unsigned lds, unsigned voff, const char* base) {
  asm volatile("s_mov_b32 m0, %0\n\ts_nop 0\n\tglobal_load_lds_dwordx4 %1, %2"
               :: "s"(lds), "v"(voff), "s"(base) : "memory");
}
__device__ __forceinline__ bf16x8 lds_b128(unsigned a) {
  return *(const LDS3 bf16x8*)(uintptr_t)a;
}
__device__ __forceinline__ bf16x8 lds_tr2(unsigned a0, unsigned a1) {
  s16x4 x = __builtin_amdgcn_ds_read_tr16_b64_v4i16((LDS3 s16x4*)(uintptr_t)a0);
  s16x4 y = __builtin_amdgcn_ds_read_tr16_b64_v4i16((LDS3 s16x4*)(uintptr_t)a1);
  s16x8 z = __builtin_shufflevector(x, y, 0, 1, 2, 3, 4, 5, 6, 7);
  return __builtin_bit_cast(bf16x8, z);
}
__device__ __forceinline__ u32x2 pack4(float a, float b, float c, float d) {
  bf16x4 p; p[0] = (__bf16)a; p[1] = (__bf16)b; p[2] = (__bf16)c; p[3] = (__bf16)d;
  return __builtin_bit_cast(u32x2, p);
}
__device__ __forceinline__ int lane_id() {
  int r; asm volatile("v_mbcnt_lo_u32_b32 %0, -1, 0\n\tv_mbcnt_hi_u32_b32 %0, -1, %0" : "=v"(r)); return r;
}
__device__ __forceinline__ unsigned addv(unsigned v, int sc) {
  unsigned r; asm volatile("v_add_u32 %0, %1, %2" : "=v"(r) : "s"(sc), "v"(v)); return r;
}
__device__ __forceinline__ void bump(unsigned& v, int sc) { asm volatile("v_add_u32 %0, %1, %0" : "+v"(v) : "s"(sc)); }
__device__ __forceinline__ unsigned x64(unsigned v) { unsigned r; asm volatile("v_xor_b32 %0, 64, %1" : "=v"(r) : "v"(v)); return r; }
__device__ __forceinline__ f32x4 zero_acc() {
  float z; asm volatile("v_mov_b32 %0, 0" : "=v"(z)); return (f32x4){z, z, z, z};
}
#if defined(__HIP_DEVICE_COMPILE__)
#define NO_PK __attribute__((target("no-packed-fp32-ops")))
#else
#define NO_PK
#endif
#define PIN(x) asm volatile("" : "+v"(x))
#define MFMA(a, b, c) __builtin_amdgcn_mfma_f32_16x16x32_bf16((a), (b), (c), 0, 0, 0)

struct K1Args {
  const char* x[2]; const char* k[2]; const char* v[2]; const char* wq[2];
  char* ows;
};

#define SB() __builtin_amdgcn_sched_barrier(0)
__device__ __forceinline__ void dma16x3(unsigned lds, unsigned v0, unsigned v1, unsigned v2, const char* base) {
  asm volatile("s_mov_b32 m0, %0\n\ts_nop 0\n\tglobal_load_lds_dwordx4 %1, %4\n\t"
               "global_load_lds_dwordx4 %2, %4 offset:1024\n\tglobal_load_lds_dwordx4 %3, %4 offset:2048"
               :: "s"(lds), "v"(v0), "v"(v1), "v"(v2), "s"(base) : "memory");
}

#define KVNT 0
template <int OFF, bool NT>
__device__ __forceinline__ void dma16o(unsigned lds, unsigned v, const char* base) {
  if (NT) asm volatile("s_mov_b32 m0, %0\n\ts_nop 0\n\tglobal_load_lds_dwordx4 %1, %2 offset:%3 nt"
                       :: "s"(lds), "v"(v), "s"(base), "n"(OFF) : "memory");
  else asm volatile("s_mov_b32 m0, %0\n\ts_nop 0\n\tglobal_load_lds_dwordx4 %1, %2 offset:%3"
                    :: "s"(lds), "v"(v), "s"(base), "n"(OFF) : "memory");
}
template <bool NT>
__device__ __forceinline__ void dma16j(int j, unsigned lds, unsigned v, const char* base) {
  if (j == 0) dma16o<0, NT>(lds, v, base); else if (j == 1) dma16o<1024, NT>(lds, v, base); else dma16o<2048, NT>(lds, v, base);
}
__device__ __forceinline__ float max3f(float a, float b, float c) { return fmaxf(fmaxf(a, b), c); }

template <int G>
__device__ __forceinline__ void k1_body(const K1Args& A, int cg, int br, int b, int h) {
  const int lane = lane_id();
  STAMP_INIT();
  constexpr int NQ = G == 0 ? 5 : 4, QT0 = G == 0 ? 0 : 5;
  constexpr int NP = G == 0 ? 4 : 5, PT0 = G == 0 ? 0 : 4;
  constexpr int NM = 5;
  constexpr int MT0 = G == 0 ? 0 : 4;
  constexpr int QO = QT0 - MT0;
  const float cs = 0.07216878364870322f * 1.4426950408889634f;
  const int li = lane & 15, g = lane >> 4;
  const unsigned fv2 = (unsigned)((((li >> 1) & 1) | ((li >> 2) & 2)) << 1);
  const unsigned fk = (unsigned)(li >> 1);
  unsigned kofs0 = (unsigned)((16 * cg + li) * 384) + (((unsigned)g ^ fv2) << 4);
  unsigned pofs0 = L_PB + (unsigned)((16 * PT0 + li) * 128) + (((unsigned)g ^ fk) << 4);
  unsigned vofs[3];
  {
    const int q = (lane >> 2) & 3, p = lane & 3;
    const unsigned fv = (unsigned)(((q >> 1) & 1) | ((g & 1) << 1));
#pragma unroll
    for (int i = 0; i < 3; ++i) {
      unsigned t = (unsigned)(3 * cg + i);
      vofs[i] = (unsigned)((8 * g + q) * 384) + ((t ^ fv) << 5) + (unsigned)(p * 8);
    }
  }
  const unsigned pw = L_PB + (unsigned)((16 * QT0 + li) * 128) + ((((unsigned)(2 * cg + (g >> 1))) ^ fk) << 4) + (unsigned)((g & 1) * 8);
  const unsigned mofs = L_M + (unsigned)((16 * MT0 + li) * 4);
  unsigned dofs[6];
  unsigned kvo[3];
  unsigned xofs = 0;
  auto mk_dofs = [&]() {
    const int ln = lane_id();
#pragma unroll
    for (int j = 0; j < 6; ++j) {
      int s = 64 * j + ln, r = (s * 2731) >> 16, c = s - r * 24;
      int f = (((r >> 1) & 1) | ((r >> 2) & 2)) << 1;
      int ch = c ^ f;
      dofs[j] = (unsigned)((16 * cg + r) * (G == 0 ? NH * DH * 2 : 384) + ch * 16 - (j % 3) * 1024);
    }
    if (G == 0) xofs = (unsigned)((ln >> 3) * (DM * 2) + (((ln & 7) ^ ((4 * (cg & 1) + (ln >> 4)) & 7)) << 4));
  };
  auto mk_kvo = [&]() {
    const int ln = lane_id();
#pragma unroll
    for (int j = 0; j < 3; ++j) {
      int s = 64 * j + ln, r = (s * 2731) >> 16, c = s - r * 24;
      kvo[j] = (unsigned)((16 * cg + r) * 384 + (c ^ (((r >> 1) & 1) << 1)) * 16 - j * 1024);
    }
  };
  mk_dofs();

  const size_t kvoff = (size_t)(b * NH + h) * SK * DH * 2;
  constexpr size_t KV4 = (size_t)4 * SK * DH * 2;
  const char* xb = A.x[br] + (size_t)b * SQ * DM * 2;
  const char* wq0 = A.wq[br] + (size_t)h * DH * 2;
  const char* kb0 = A.k[br] + kvoff;
  const char* vb0 = A.v[br] + kvoff;
  auto issue_unit = [&](const char* src, int unit) {
    dma16x3((unsigned)(unit * UNIT + cg * 6144), dofs[0], dofs[1], dofs[2], src);
    dma16x3((unsigned)(unit * UNIT + cg * 6144 + 3072), dofs[3], dofs[4], dofs[5], src);
  };
  auto issue_kv = [&](const char* src, int unit) {
    if (G == 0) dma16x3((unsigned)(unit * UNIT + cg * 6144), kvo[0], kvo[1], kvo[2], src);
    else dma16x3((unsigned)(unit * UNIT + cg * 6144 + 3072), dofs[3], dofs[4], dofs[5], src);
  };
  auto unit1 = [&](const char* src, int unit, int j) {
    dma16j<G == 1 && KVNT>(j % 3, (unsigned)(unit * UNIT + cg * 6144 + (j / 3) * 3072), dofs[j], src);
  };
  auto kv1 = [&](const char* src, int unit, int j) {
    if (G == 0) dma16j<KVNT>(j, (unsigned)(unit * UNIT + cg * 6144), kvo[j], src);
    else dma16j<KVNT>(j, (unsigned)(unit * UNIT + cg * 6144 + 3072), dofs[3 + j], src);
  };
  auto x1 = [&](const char* src, int xo, int i) {
    int p = cg + 4 * i; if (p > 17) p -= 2;
    dma16((unsigned)(L_PB + xo + p * 1024), xofs, src + (size_t)p * 8 * DM * 2);
  };
  auto issue_x = [&](const char* src, int pb) {
#pragma unroll
    for (int i = 0; i < 5; ++i) {
      int p = cg + 4 * i; if (p > 17) p -= 2;
      dma16((unsigned)(L_PB + pb * PBUF + p * 1024), xofs, src + (size_t)p * 8 * DM * 2);
    }
  };

#define PRV 3
#define PRIO(v, p) do { if (PRV == (v)) __builtin_amdgcn_s_setprio(p); } while (0)
  if (G == 1) PRIO(0, 1);
  if (G == 0) {
    const char* wb = wq0;
    issue_unit(wb, 2);
    issue_x(xb, 0);
    issue_unit(wb + (size_t)64 * NH * DH * 2, 3);
    issue_unit(wb + (size_t)128 * NH * DH * 2, 4);
    WAIT_VM(12);
  } else {
    issue_unit(kb0, 1);
    if (cg == 0 && lane < 2) *(LDS3 int*)(uintptr_t)(L_F + lane * 4) = 0;
  }
  BAR();

  int cv = 0, cp = 0, ck = 0;
  auto set_v = [&](int unit) { const int o = unit * UNIT; bump(vofs[0], o - cv); bump(vofs[1], o - cv); bump(vofs[2], o - cv); cv = o; };
  auto set_p = [&](int o) { bump(pofs0, o - cp); cp = o; };
  auto set_k = [&](int unit) { const int o = unit * UNIT; bump(kofs0, o - ck); ck = o; };
  f32x4 acc[NP][3];
  bf16x8 fa[2][3], fb[2][NP], fr[4];
  auto qphase = [&](int km, int unit, int xo, bool mm, bool rd, auto&& slot) {
    if (km == 1 && rd) { set_v(unit); set_p(xo); }
    const unsigned pa = km ? pofs0 : x64(pofs0);
#pragma unroll
    for (int m = 0; m < 3 * NP; ++m) {
      if (mm) acc[m / 3][m % 3] = MFMA(fa[km][m % 3], fb[km][m / 3], acc[m / 3][m % 3]);
      SB();
      if (rd) {
        if (m < 3) {
          const unsigned va = vofs[m] + (1 - km) * 12288;
          fa[1 - km][m] = lds_tr2(va, va + 1536);
        } else if (m < 3 + NP) fb[1 - km][m - 3] = lds_b128(pa + (m - 3) * 2048);
      }
      slot(m);
      SB();
    }
  };
  auto noslot = [&](int) {};

  for (int it = 0; it < 2; ++it) {
    const char* wb = wq0 + it * 4 * DH * 2;
    const char* kb = kb0 + it * KV4;
    const char* vb = vb0 + it * KV4;
#pragma unroll
    for (int t = 0; t < NP; ++t)
#pragma unroll
      for (int i = 0; i < 3; ++i) acc[t][i] = zero_acc();
    {
      int tid = G * 256 + cg * 64 + lane_id();
      if (tid < 2 * SQ) *(LDS3 float*)(uintptr_t)(L_M + tid * 4) = -__builtin_inff();
    }
    qphase(1, 2, 0, false, true, [&](int m) { if (G == 0 && m >= 3 && m < 8) x1(xb + 128, PBUF, m - 3); });
    qphase(0, 2, 0, true, true, [&](int m) { if (G == 0 && (m & 1) && m < 10) x1(xb + 256, -L_PB, m >> 1); });
    if (G == 0) WAIT_VM(5);
    BAR();
    int wu = 3;
#pragma unroll 1
    for (int j = 1; j <= 7; ++j) {
      const char* wn = wb + (size_t)(j + 2) * 64 * NH * DH * 2;
      const int un = wu == 2 ? 4 : wu - 1;
      const int xo = wu == 4 ? -L_PB : (wu - 2) * PBUF, xn = un == 4 ? -L_PB : (un - 2) * PBUF;
      qphase(1, wu, xo, true, true, [&](int m) { if (G == 0 && m < 10 && (m & 1)) x1(xb + (j + 2) * 128, xn, m >> 1); if (G == 0 && m == 11) unit1(wn, un, 0); });
      qphase(0, wu, xo, true, true, [&](int m) { if (G == 0 && !(m & 1) && m < 10) unit1(wn, un, 1 + (m >> 1)); });
      if (G == 0) WAIT_VM(11);
      BAR();
      wu = wu == 4 ? 2 : wu + 1;
    }
    qphase(1, 4, -L_PB, true, true, noslot);
    qphase(0, 4, -L_PB, true, true, noslot);
    if (G == 0) WAIT_VM(0); else mk_dofs();
    BAR();
    qphase(1, 2, 0, true, true, [&](int m) { if (G == 1 && (m & 1) && m < 12) unit1(kb + UNIT, 0, m >> 1); });
    qphase(0, 2, 0, true, true, [&](int m) { if (G == 1 && (m & 1) && m < 12) unit1(kb + 2 * UNIT, 3, m >> 1); });
    BAR();
    if (G == 0) mk_kvo();
    qphase(1, 0, 0, true, false, [&](int m) { if (m % 3 == 1 && m < 9) kv1(vb, 2, m / 3); });
    const int lq = lane_id(), liq = lq & 15, gq = lq >> 4;
    const unsigned fkq = (unsigned)(liq >> 1);
#pragma unroll
    for (int t = 0; t < NP; ++t)
#pragma unroll
      for (int i = 0; i < 3; ++i) {
        unsigned ch = (unsigned)(2 * (3 * cg + i) + (gq >> 1));
        unsigned a = L_Q + (unsigned)((16 * (PT0 + t) + liq) * 384) + ((ch ^ fkq) << 4) + (unsigned)((gq & 1) * 8);
        *(LDS3 u32x2*)(uintptr_t)a = pack4(acc[t][i][0] * cs, acc[t][i][1] * cs, acc[t][i][2] * cs, acc[t][i][3] * cs);
        acc[t][i] = zero_acc();
      }
    BAR();
    bf16x8 qf[NQ][6];
    {
      unsigned q0 = L_Q + (unsigned)((16 * QT0 + liq) * 384) + (((unsigned)gq ^ fkq) << 4);
#pragma unroll
      for (int t = 0; t < NQ; ++t)
#pragma unroll
        for (int ks = 0; ks < 6; ++ks) qf[t][ks] = lds_b128(((ks & 1) ? (q0 ^ 64u) : q0) + t * 6144 + (ks >> 1) * 128);
    }
    if (G == 1) WAIT_VM(15);
    BAR();
    issue_kv(vb + UNIT, 4);

    constexpr float THR = 32.0f, ZR = 24.0f;
    bool nz = false;
    unsigned k1a = 0, p1a = 0;
    f32x4 s[NQ];
    const f32x4 zero4 = {0.f, 0.f, 0.f, 0.f};
    float mc[NM], lsum[NQ], alpha[NP];
    bf16x8 kf[3];
    int flg = 0;
    bool pend = false;
#pragma unroll
    for (int t = 0; t < NM; ++t) mc[t] = 0.f;
#pragma unroll
    for (int t = 0; t < NQ; ++t) lsum[t] = 0.f;
#pragma unroll
    for (int t = 0; t < NP; ++t) alpha[t] = 1.f;

    auto qk_pre = [&](int unit) {
      set_k(unit);
      k1a = x64(kofs0);
      kf[0] = lds_b128(kofs0);
      kf[1] = lds_b128(k1a);
    };
    auto qk = [&](int unit, auto&& slot) {
#pragma unroll
      for (int ks = 0; ks < 6; ++ks) {
#pragma unroll
        for (int t = 0; t < NQ; ++t) {
          s[t] = MFMA(kf[ks % 3], qf[t][ks], ks == 0 ? zero4 : s[t]);
          SB();
          if (t == 0 && ks + 2 < 6) kf[(ks + 2) % 3] = lds_b128(((ks & 1) ? k1a : kofs0) + ((ks + 2) >> 1) * 128);
          if (t >= 1) slot(ks * 4 + t);
          SB();
        }
      }
    };
    auto check = [&](int par, int gen, bool first) {
      if (nz) {
#pragma unroll
        for (int t = 0; t < NQ; ++t)
#pragma unroll
          for (int e = 0; e < 4; ++e) s[t][e] += mc[QO + t];
      }
      float mx = fmaxf(s[0][0], s[0][1]);
#pragma unroll
      for (int t = 0; t < NQ; ++t) mx = t ? max3f(max3f(mx, s[t][0], s[t][1]), s[t][2], s[t][3]) : max3f(mx, s[0][2], s[0][3]);
      if (first || __builtin_amdgcn_ballot_w64(!(mx <= THR)) != 0) {
        const unsigned mb = addv(mofs, par * SQ * 4);
#pragma unroll
        for (int t = 0; t < NQ; ++t) {
          float y = fmaxf(fmaxf(s[t][0], s[t][1]), fmaxf(s[t][2], s[t][3])) - mc[QO + t];
          __hip_atomic_fetch_max((LDS3 float*)(uintptr_t)(mb + (unsigned)((QO + t) * 64)), y,
                                 __ATOMIC_RELAXED, __HIP_MEMORY_SCOPE_WORKGROUP);
        }
        *(volatile LDS3 int*)(uintptr_t)(L_F + par * 4) = gen;
      }
    };
    auto ld_flag = [&](int par) { flg = *(volatile LDS3 int*)(uintptr_t)(L_F + par * 4); };
    auto update = [&](int par, int gen, bool first) {
      if (first || __builtin_amdgcn_readfirstlane(flg) == gen) {
        const unsigned mb = addv(mofs, par * SQ * 4);
#pragma unroll
        for (int t = 0; t < NM; ++t) {
          float m = *(const LDS3 float*)(uintptr_t)(mb + (unsigned)(t * 64));
          float mn = first ? (fabsf(m) <= ZR ? 0.f : -m) : fminf(mc[t], -m);
          float d = mn - mc[t];
          mc[t] = mn;
          float al = __builtin_amdgcn_exp2f(d);
          if (t >= QO && t < QO + NQ) {
#pragma unroll
            for (int e = 0; e < 4; ++e) s[t - QO][e] += d;
            if (!first) lsum[t - QO] *= al;
          }
          if (!first && t < NP) alpha[t] = al;
        }
        if (!first) { pend = true; nz = true; }
        else {
          bool z = true;
#pragma unroll
          for (int t = 0; t < NQ; ++t) z = z && mc[QO + t] == 0.f;
          nz = __builtin_amdgcn_ballot_w64(!z) != 0;
        }
      }
    };
    auto softmax = [&](int par, auto&& slot) {
      const unsigned pwb = addv(pw, par * PBUF);
#pragma unroll
      for (int t = 0; t < NQ; ++t) {
        float p0 = __builtin_amdgcn_exp2f(s[t][0]);
        float p1 = __builtin_amdgcn_exp2f(s[t][1]);
        SB();
        slot(t);
        SB();
        float p2 = __builtin_amdgcn_exp2f(s[t][2]);
        float p3 = __builtin_amdgcn_exp2f(s[t][3]);
        lsum[t] += (p0 + p1) + (p2 + p3);
        PIN(lsum[t]);
        *(LDS3 u32x2*)(uintptr_t)(pwb + (unsigned)(t * 2048)) = pack4(p0, p1, p2, p3);
        SB();
      }
    };
    auto rescale = [&]() {
      if (pend) {
        asm volatile("s_nop 7\n\ts_nop 7\n\ts_nop 3" ::: "memory");
#pragma unroll
        for (int t = 0; t < NP; ++t)
#pragma unroll
          for (int i = 0; i < 3; ++i) {
#pragma unroll
            for (int e = 0; e < 4; ++e) {
              float v = acc[t][i][e];
              asm volatile("v_mul_f32 %0, %0, %1" : "+v"(v) : "v"(alpha[t]));
              acc[t][i][e] = v;
            }
          }
        asm volatile("s_nop 1" ::: "memory");
        pend = false;
      }
    };
    auto ld_v = [&](int ks, int unit, int i) {
      if (ks == 0 && i == 0) set_v(unit);
      const unsigned va = vofs[i] + ks * 12288;
      fa[ks][i] = lds_tr2(va, va + 1536);
    };
    auto rs = [&](int st, int sh) { return (st >= NP ? st + sh : st) & 3; };
    constexpr int SH = G == 0 ? 0 : 2;
    auto ld_p = [&](int par, int st, int sh = G == 0 ? 0 : 2) {
      if (st == 0) set_p(par * PBUF);
      if (st == NP) p1a = x64(pofs0);
      fr[rs(st, sh)] = lds_b128((st >= NP ? p1a : pofs0) + (st % NP) * 2048);
    };
    auto pv_steps = [&](int s0, int s1, int sh, auto&& slot) {
#pragma unroll
      for (int st = s0; st < s1; ++st)
#pragma unroll
        for (int i = 0; i < 3; ++i) {
          acc[st % NP][i] = MFMA(fa[st / NP][i], fr[rs(st, sh)], acc[st % NP][i]);
          SB();
          slot(st * 3 + i);
          SB();
        }
    };
    const int gen0 = it * 8 + 1;

    qk_pre(1);
    qk(1, noslot);
    check(0, gen0, true);
    if (G == 1) WAIT_VM(12);
    BAR();
    qk_pre(0);
    update(0, gen0, true);
    SB();
    softmax(0, noslot);
    qk(0, noslot);
    check(1, gen0 + 1, false);
    WAIT_VM(3);
    BAR();
    int uv = 2;
    {
#pragma unroll
      for (int i = 0; i < 3; ++i) fa[1][i] = __builtin_bit_cast(bf16x8, zero_acc());
#pragma unroll
      for (int i = 0; i < 4; ++i) fr[i] = __builtin_bit_cast(bf16x8, zero_acc());
    }
    const char* wnx = wq0 + 4 * DH * 2;
    auto interval = [&](int c, auto kind_c) {
      constexpr int KIND = decltype(kind_c)::value;
      constexpr bool LAST = KIND == 3;
      const int uk = uv == 4 ? 0 : uv + 1;
      const int u3 = uv >= 2 ? uv - 2 : uv + 3;
      const int u4 = uv >= 1 ? uv - 1 : uv + 4;
      const int pp = (c - 1) & 1;
      const char* src3 = KIND <= 1 ? kb + (size_t)(c + 2) * UNIT : vb + (size_t)7 * UNIT;
      const char* src4 = vb + (size_t)(c + 1) * UNIT;
      auto d3 = [&](int j) { if (KIND == 1 || KIND == 2) kv1(src3, u3, j); };
      auto d4 = [&](int j) { if (KIND <= 1) kv1(src4, u4, j); };
      if (G == 0) {
        ld_flag(c & 1);
        pv_steps(6, 8, SH, [&](int m) {
          if (m == 18) ld_v(0, uv, 0);
          if (m == 19) ld_p(pp, 0);
          if (m == 20) ld_v(0, uv, 1);
          if (m == 21) ld_v(0, uv, 2);
          if (m == 22) ld_p(pp, 1);
        });
        if (LAST) mk_dofs();
        rescale();
        update(c & 1, gen0 + c, false);
        SB();
        PRIO(2, 1); PRIO(3, 1);
        softmax(c & 1, [&](int t) { if (LAST) { if (it == 0) unit1(wnx, 2, t); } else if (t < 3) d3(t); });
        PRIO(2, 0); PRIO(3, 0);
        pv_steps(0, LAST ? 6 : 5, SH, [&](int m) {
          const int st = m / 3, i = m % 3;
          if (i == 0 && st + 2 < (LAST ? 8 : 6)) ld_p(pp, st + 2);
          if (i == 1 && st < 3) ld_v(1, uv, st);
          if (i == 2 && st < 3) d4(st);
          if (!LAST && m == 10) qk_pre(uk);
          if (LAST && it == 0) {
            if (m == 2) unit1(wnx, 2, 5);
            if (m >= 5 && m < 17 && (m & 1)) unit1(wnx + (size_t)64 * NH * DH * 2, 3, (m - 5) >> 1);
          }
        });
        if (!LAST) {
          qk(uk, [&](int m) { if (m == 18) ld_p(pp, 6); if (m == 22) ld_p(pp, 7); });
          pv_steps(5, 6, SH, noslot);
          check((c + 1) & 1, gen0 + c + 1, false);
          WAIT_VM(3);
        }
      } else {
        PRIO(1, 1);
        ld_flag(c & 1);
        pv_steps(7, 10, SH, [&](int m) {
          if (m == 21) ld_v(0, uv, 0);
          if (m == 22) ld_p(pp, 0);
          if (m == 23) ld_v(0, uv, 1);
          if (m == 24) ld_v(0, uv, 2);
          if (m == 25) ld_p(pp, 1);
          if (m == 26) d3(0);
          if (m == 27) ld_p(pp, 2);
          if (m == 28) d3(1);
        });
        rescale();
        pv_steps(0, 5, SH, [&](int m) {
          if (m == 0) ld_p(pp, 3);
          if (m == 1) d3(2);
          if (m == 3) ld_p(pp, 4);
          if (!LAST && m == 10) qk_pre(uk);
        });
        update(c & 1, gen0 + c, false);
        SB();
        PRIO(2, 1); PRIO(3, 1);
        softmax(c & 1, [&](int t) { if (t < 3) d4(t); });
        PRIO(1, 0); PRIO(2, 0);
        if (KIND == 2 && it == 0) { mk_dofs(); issue_unit(kb0 + KV4, u4); }
        if (!LAST) {
          qk(uk, [&](int m) {
            if (m == 13) ld_v(1, uv, 0);
            if (m == 14) ld_p(pp, 5);
            if (m == 17) ld_v(1, uv, 1);
            if (m == 18) ld_v(1, uv, 2);
            if (m == 21) ld_p(pp, 6);
            if (m == 22) ld_p(pp, 7);
          });
          pv_steps(5, 6, SH, [&](int m) { if (m == 15) ld_p(pp, 8); });
          check((c + 1) & 1, gen0 + c + 1, false);
        } else {
          ld_v(1, uv, 0); ld_p(pp, 5); ld_v(1, uv, 1); ld_v(1, uv, 2); ld_p(pp, 6); ld_p(pp, 7);
          pv_steps(5, 6, SH, [&](int m) { if (m == 15) ld_p(pp, 8); });
        }
        SB();
        PRIO(3, 0);
        pv_steps(6, 7, SH, [&](int m) { if (m == 18) ld_p(pp, 9); });
        if (!LAST) { if (KIND == 2 && it == 0) WAIT_VM(9); else WAIT_VM(3); }
      }
      uv = uv >= 3 ? uv - 3 : uv + 2;
    };
#pragma unroll 1
    for (int c = 1; c <= 5; ++c) {
      interval(c, std::integral_constant<int, 1>{});
      BAR();
    }
    interval(6, std::integral_constant<int, 2>{});
    BAR();
    interval(7, std::integral_constant<int, 3>{});
    const int le = lane_id();
#pragma unroll
    for (int t = 0; t < NQ; ++t) {
      float l = lsum[t];
      l += __builtin_bit_cast(float, __builtin_amdgcn_ds_bpermute((le ^ 16) << 2, __builtin_bit_cast(int, l)));
      l += __builtin_bit_cast(float, __builtin_amdgcn_ds_bpermute((le ^ 32) << 2, __builtin_bit_cast(int, l)));
      if (le < 16) *(LDS3 float*)(uintptr_t)(L_L + (unsigned)(cg * SQ * 4 + (16 * (QT0 + t) + le) * 4)) = l;
    }
    if (G == 0) { if (it == 0) WAIT_VM(12); else WAIT_VM(0); }
    else { if (it == 0) WAIT_VM(6); else WAIT_VM(0); }
    BAR();
    if (G == 0) {
      if (it == 0) issue_x(xb, 0);
      pv_steps(6, 8, SH, [&](int m) {
        if (m == 18) ld_v(0, 0, 0);
        if (m == 19) ld_p(1, 0);
        if (m == 20) ld_v(0, 0, 1);
        if (m == 21) ld_v(0, 0, 2);
        if (m == 22) ld_p(1, 1);
      });
      rescale();
      pv_steps(0, 8, SH, [&](int m) {
        const int st = m / 3, i = m % 3;
        if (i == 0 && st + 2 < 8) ld_p(1, st + 2);
        if (i == 1 && st < 3) ld_v(1, 0, st);
        if (it == 0 && i == 2 && st < 6) unit1(wnx + (size_t)128 * NH * DH * 2, 4, st);
      });
    } else {
      pv_steps(7, 10, SH, [&](int m) {
        if (m == 21) ld_v(0, 0, 0);
        if (m == 22) ld_p(1, 0);
        if (m == 23) ld_v(0, 0, 1);
        if (m == 24) ld_v(0, 0, 2);
        if (m == 25) ld_p(1, 1);
        if (m == 27) ld_p(1, 2);
      });
      rescale();
      pv_steps(0, 10, 0, [&](int m) {
        const int st = m / 3, i = m % 3;
        if (i == 0 && st + 3 < 10) ld_p(1, st + 3, 0);
        if (i == 1 && st < 3) ld_v(1, 0, st);
      });
    }
    {
      const __amdgpu_buffer_rsrc_t ors =
          __builtin_amdgcn_make_buffer_rsrc(A.ows + (size_t)((br * NB + b) * NH + h + 4 * it) * OWS_ITEM, 0, (int)OWS_ITEM, 0x00020000);
      const int ufull = (3 * cg + 1) >> 1;
      const int uhalf = (cg & 1) ? (3 * cg) >> 1 : (3 * cg + 2) >> 1;
#pragma unroll
      for (int t = 0; t < NP; ++t) {
        unsigned la = L_L + (unsigned)((16 * (PT0 + t) + (le & 15)) * 4);
        float l = (*(const LDS3 float*)(uintptr_t)la + *(const LDS3 float*)(uintptr_t)(la + SQ * 4)) +
                  (*(const LDS3 float*)(uintptr_t)(la + 2 * SQ * 4) + *(const LDS3 float*)(uintptr_t)(la + 3 * SQ * 4));
        float inv = 1.0f / l;
        u32x2 o[3];
#pragma unroll
        for (int i = 0; i < 3; ++i) o[i] = pack4(acc[t][i][0] * inv, acc[t][i][1] * inv, acc[t][i][2] * inv, acc[t][i][3] * inv);
        u32x4 full; u32x2 half;
        if (cg & 1) { full = (u32x4){o[1][0], o[1][1], o[2][0], o[2][1]}; half = o[0]; }
        else        { full = (u32x4){o[0][0], o[0][1], o[1][0], o[1][1]}; half = o[2]; }
#define OWT 0
        __builtin_amdgcn_raw_buffer_store_b128(full, ors, le * 16 + t * 2048, (ufull >> 1) * PBUF + PT0 * 2048 + (ufull & 1) * 1024, OWT);
        __builtin_amdgcn_raw_buffer_store_b64(half, ors, le * 16 + t * 2048, (uhalf >> 1) * PBUF + PT0 * 2048 + (uhalf & 1) * 1024 + (cg & 1) * 8, OWT);
      }
    }
    if (G == 0 && it == 0) WAIT_VM(14);
    BAR();
  }
}

NO_PK __global__ __launch_bounds__(512, 2) void k1_attn(K1Args A) {
  extern __shared__ __attribute__((aligned(16))) char shm[];
  const int w = __builtin_amdgcn_readfirstlane(threadIdx.x >> 6);
  const int xcd = blockIdx.x & 7, idx = blockIdx.x >> 3;
  const int br = xcd >> 2, h = (xcd & 3) * 8 + (idx & 3), b = idx >> 2;
  if (w < 4) k1_body<0>(A, w, br, b, h);
  else k1_body<1>(A, w - 4, br, b, h);
}

constexpr int K2_STAGE = PBUF + 64 * 320;
constexpr int K2_NST = 4;
constexpr int K2_CH = 24;
struct K2Args { const char* wo[2]; const char* ows; float* part; };

template <int NR, int NC, bool WARM>
__device__ __forceinline__ void k2_body(const K2Args& A, int w, int lane, int rt0, int ct0, int br, int b, int nb, int sp) {
  STAMP_INIT();
  const int g = lane >> 4;
  unsigned dofs[6]; int dlds[6]; bool isw[6];
#pragma unroll
  for (int i = 0; i < 6; ++i) {
    int p = (w < 4 ? w : w - 1) + 7 * i; if (p >= 38) p -= 4;
    isw[i] = p >= 18;
    dlds[i] = p * 1024;
    if (p < 18) dofs[i] = (unsigned)(p * 1024 + lane * 16);
    else {
      int s = 64 * (p - 18) + lane, r = s / 20, c = s - r * 20;
      dofs[i] = (unsigned)(r * (DM * 2) + ((c ^ (((r >> 2) & 1) << 1)) << 4));
    }
  }
  const char* ob = A.ows + ((size_t)(br * NB + b) * NH * 3 + (size_t)sp * K2_CH) * PBUF;
  const char* wb = A.wo[br] + (size_t)sp * K2_CH * 64 * DM * 2 + nb * 320;
  auto issue1 = [&](int ci, int i) {
    if (!WARM) dma16((unsigned)((ci & 3) * K2_STAGE) + (unsigned)dlds[i], dofs[i], isw[i] ? wb + (size_t)ci * 64 * DM * 2 : ob + (size_t)ci * PBUF);
  };
  auto issue = [&](int ci) {
#pragma unroll
    for (int i = 0; i < 6; ++i) issue1(ci, i);
  };
#define WD 2
  const int wl = lane < 36 ? lane : 35, wr = lane < 24 ? lane : 23;
  const unsigned wo1 = (unsigned)(nb * 4608 + wl * 128), wo2 = (unsigned)((8 * b + wr / 3) * (DM * 2) + (wr % 3) * 128);
  auto warm = [&](int c) {
    if (WARM && c < K2_CH)
      asm volatile("s_mov_b32 m0, %0\n\ts_nop 0\n\tglobal_load_lds_dword %1, %2\n\tglobal_load_lds_dword %3, %4"
                   :: "s"(K2_NST * K2_STAGE), "v"(wo1), "s"(ob + (size_t)c * PBUF), "v"(wo2), "s"(wb + (size_t)c * 64 * DM * 2) : "memory");
  };
  unsigned aofs[NC];
  {
    const int q = (lane >> 2) & 3, p = lane & 3;
#pragma unroll
    for (int i = 0; i < NC; ++i) aofs[i] = PBUF + (unsigned)((4 * g + q) * 320 + (((ct0 + i) ^ (g & 1)) << 5) + p * 8);
  }
  const unsigned bofs = (unsigned)(rt0 * 2048 + lane * 16);
  f32x4 acc[NR][NC];
#pragma unroll
  for (int t = 0; t < NR; ++t)
#pragma unroll
    for (int i = 0; i < NC; ++i) acc[t][i] = (f32x4){0.f, 0.f, 0.f, 0.f};
  bf16x8 fa[2][NC], fb[2][NR];
  auto ld_one = [&](int ks, unsigned base, int r) {
    if (r < NC) fa[ks][r] = lds_tr2(base + aofs[r] + ks * 10240, base + aofs[r] + ks * 10240 + 5120);
    else if (r < NC + NR) fb[ks][r - NC] = lds_b128(base + bofs + (r - NC) * 2048 + ks * 1024);
  };
  auto phase = [&](int km, unsigned base, bool rd, int cd, int d0, int nd) {
#pragma unroll
    for (int m = 0; m < NR * NC; ++m) {
      acc[m / NC][m % NC] = MFMA(fa[km][m % NC], fb[km][m / NC], acc[m / NC][m % NC]);
      SB();
      if (rd) ld_one(1 - km, base, m);
      constexpr int GAP = NR * NC >= 12 ? 4 : 3;
      if (cd >= 0 && m % GAP == 1 && m / GAP < nd) issue1(cd, d0 + m / GAP);
      SB();
    }
  };
  issue(0); issue(1); issue(2);
#pragma unroll
  for (int i = 3; i < 3 + WD; ++i) warm(i);
  if (!WARM) WAIT_VM(12);
  BAR();
#pragma unroll
  for (int r = 0; r < NC + NR; ++r) ld_one(0, 0u, r);
  SB();
  issue(3);
  SB();
  phase(0, 0u, true, -1, 0, 0);
  warm(3 + WD);
  if (!WARM) WAIT_VM(12);
  BAR();
  for (int ci = 1; ci < K2_CH - 3; ++ci) {
    unsigned base = (unsigned)((ci & 3) * K2_STAGE);
    phase(1, base, true, ci + 3, 0, 3);
    phase(0, base, true, ci + 3, 3, 3);
    warm(ci + 3 + WD);
    if (!WARM) WAIT_VM(12);
    BAR();
  }
#pragma unroll
  for (int ci = K2_CH - 3; ci < K2_CH; ++ci) {
    unsigned base = (unsigned)((ci & 3) * K2_STAGE);
    phase(1, base, true, -1, 0, 0);
    phase(0, base, true, -1, 0, 0);
    if (ci == K2_CH - 3) { if (!WARM) WAIT_VM(6); BAR(); } else if (ci == K2_CH - 2) { WAIT_VM(0); BAR(); }
  }
  phase(1, 0u, false, -1, 0, 0);
  float* pb = A.part + ((size_t)(((br * NB + b) * 4 + nb) * 4 + sp) * 90) * 256 + lane * 4;
#pragma unroll
  for (int t = 0; t < NR; ++t)
#pragma unroll
    for (int i = 0; i < NC; ++i) *(f32x4*)(pb + ((rt0 + t) * 10 + ct0 + i) * 256) = acc[t][i];
}

__global__ __launch_bounds__(512, 2) void k2_oproj(K2Args A) {
  extern __shared__ __attribute__((aligned(16))) char shm[];
  const int w = __builtin_amdgcn_readfirstlane(threadIdx.x >> 6);
  const int lane = threadIdx.x & 63;
  const int xcd = blockIdx.x & 7, idx = blockIdx.x >> 3;
  const int br = xcd >> 2, sp = xcd & 3, b = idx >> 2, nb = idx & 3;
  const int cg = w & 3;
  if (w < 2) k2_body<5, 3, false>(A, w, lane, 0, 3 * cg, br, b, nb, sp);
  else if (w < 4) k2_body<5, 2, false>(A, w, lane, 0, 2 * cg + 2, br, b, nb, sp);
  else if (w == 4) k2_body<4, 2, true>(A, w, lane, 5, 2 * cg, br, b, nb, sp);
  else if (w == 5) k2_body<4, 2, false>(A, w, lane, 5, 2 * cg, br, b, nb, sp);
  else k2_body<4, 3, false>(A, w, lane, 5, 3 * cg - 2, br, b, nb, sp);
}

__global__ __launch_bounds__(256) void k3_reduce(const float* __restrict__ part, char* __restrict__ out) {
  int gid = blockIdx.x * 256 + threadIdx.x;
  int lane = gid & 63, f = (gid >> 6) % 90, tile = (gid >> 6) / 90;
  const float* p = part + ((size_t)tile * 4 * 90 + f) * 256 + lane * 4;
  f32x4 s = *(const f32x4*)p;
  s += *(const f32x4*)(p + 90 * 256);
  s += *(const f32x4*)(p + 2 * 90 * 256);
  s += *(const f32x4*)(p + 3 * 90 * 256);
  int nb = tile & 3, b = (tile >> 2) & 7, br = tile >> 5;
  int rt = f / 10, ct = f - rt * 10;
  int q = rt * 16 + (lane & 15), n = br * DM + nb * 160 + ct * 16 + (lane >> 4) * 4;
  *(u32x2*)(out + ((size_t)(b * SQ + q) * (2 * DM) + n) * 2) = pack4(s[0], s[1], s[2], s[3]);
}

extern "C" void kernel_launch(void* const* d_in, const int* in_sizes, int n_in, void* d_out, int out_size,
                              void* d_ws, size_t ws_size, hipStream_t stream) {
  (void)in_sizes; (void)n_in; (void)out_size; (void)ws_size;
  K1Args a1;
  for (int i = 0; i < 2; ++i) {
    a1.x[i] = (const char*)d_in[5 * i + 0];
    a1.k[i] = (const char*)d_in[5 * i + 1];
    a1.v[i] = (const char*)d_in[5 * i + 2];
    a1.wq[i] = (const char*)d_in[5 * i + 3];
  }
  a1.ows = (char*)d_ws;
  K2Args a2;
  a2.wo[0] = (const char*)d_in[4];
  a2.wo[1] = (const char*)d_in[9];
  a2.ows = (const char*)d_ws;
  a2.part = (float*)((char*)d_ws + OWS_BYTES);
  (void)hipFuncSetAttribute((const void*)k1_attn, hipFuncAttributeMaxDynamicSharedMemorySize, L_TOT1);
  (void)hipFuncSetAttribute((const void*)k2_oproj, hipFuncAttributeMaxDynamicSharedMemorySize, K2_NST * K2_STAGE + 256);
  k1_attn<<<256, 512, L_TOT1, stream>>>(a1);
  k2_oproj<<<256, 512, K2_NST * K2_STAGE + 256, stream>>>(a2);
  k3_reduce<<<64 * 90 * 64 / 256, 256, 0, stream>>>(a2.part, (char*)d_out);
}
```

```cpp
#include <hip/hip_runtime.h>
#include <hip/hip_bf16.h>
#include <stdint.h>
#include <type_traits>

typedef __attribute__((ext_vector_type(4))) short s16x4;
typedef __attribute__((ext_vector_type(8))) short s16x8;
typedef __attribute__((ext_vector_type(8))) __bf16 bf16x8;
typedef __attribute__((ext_vector_type(4))) __bf16 bf16x4;
typedef __attribute__((ext_vector_type(4))) float f32x4;
typedef __attribute__((ext_vector_type(2))) float f32x2;
typedef __attribute__((ext_vector_type(2))) unsigned u32x2;
typedef __attribute__((ext_vector_type(4))) unsigned u32x4;
#define LDS3 __attribute__((address_space(3)))

constexpr int NB = 8, SQ = 144, DM = 640, NH = 32, SK = 512, DH = 192;
constexpr int UNIT = 24576;
constexpr int PBUF = 18432;
constexpr int L_PB = 5 * UNIT;
constexpr int L_M = L_PB + 2 * PBUF;
constexpr int L_L = L_M + 2 * SQ * 4;
constexpr int L_F = L_L + 4 * SQ * 4;
constexpr int L_PF = L_F + 16;
constexpr int L_TOT1 = L_PF + 256;
constexpr int L_Q = 4 * UNIT;
constexpr size_t OWS_ITEM = 55296;
constexpr size_t OWS_BYTES = 512 * OWS_ITEM;

#define WAIT_VM(n) asm volatile("s_waitcnt vmcnt(" #n ")" ::: "memory")
#define STAMP_INIT() do {} while (0)
#define STAMP() do {} while (0)
#define BAR() do { asm volatile("" ::: "memory"); __builtin_amdgcn_s_waitcnt(0xC07F); __builtin_amdgcn_s_barrier(); asm volatile("" ::: "memory"); STAMP(); } while (0)

__device__ __forceinline__ void dma16(unsigned lds, unsigned voff, const char* base) {
  asm volatile("s_mov_b32 m0, %0\n\ts_nop 0\n\tglobal_load_lds_dwordx4 %1, %2"
               :: "s"(lds), "v"(voff), "s"(base) : "memory");
}
__device__ __forceinline__ bf16x8 lds_b128(unsigned a) {
  return *(const LDS3 bf16x8*)(uintptr_t)a;
}
__device__ __forceinline__ bf16x8 lds_tr2(unsigned a0, unsigned a1) {
  s16x4 x = __builtin_amdgcn_ds_read_tr16_b64_v4i16((LDS3 s16x4*)(uintptr_t)a0);
  s16x4 y = __builtin_amdgcn_ds_read_tr16_b64_v4i16((LDS3 s16x4*)(uintptr_t)a1);
  s16x8 z = __builtin_shufflevector(x, y, 0, 1, 2, 3, 4, 5, 6, 7);
  return __builtin_bit_cast(bf16x8, z);
}
__device__ __forceinline__ u32x2 pack4(float a, float b, float c, float d) {
  bf16x4 p; p[0] = (__bf16)a; p[1] = (__bf16)b; p[2] = (__bf16)c; p[3] = (__bf16)d;
  return __builtin_bit_cast(u32x2, p);
}
__device__ __forceinline__ int lane_id() {
  int r; asm volatile("v_mbcnt_lo_u32_b32 %0, -1, 0\n\tv_mbcnt_hi_u32_b32 %0, -1, %0" : "=v"(r)); return r;
}
__device__ __forceinline__ unsigned addv(unsigned v, int sc) {
  unsigned r; asm volatile("v_add_u32 %0, %1, %2" : "=v"(r) : "s"(sc), "v"(v)); return r;
}
__device__ __forceinline__ void bump(unsigned& v, int sc) { asm volatile("v_add_u32 %0, %1, %0" : "+v"(v) : "s"(sc)); }
__device__ __forceinline__ unsigned x64(unsigned v) { unsigned r; asm volatile("v_xor_b32 %0, 64, %1" : "=v"(r) : "v"(v)); return r; }
__device__ __forceinline__ f32x4 zero_acc() {
  float z; asm volatile("v_mov_b32 %0, 0" : "=v"(z)); return (f32x4){z, z, z, z};
}
#if defined(__HIP_DEVICE_COMPILE__)
#define NO_PK __attribute__((target("no-packed-fp32-ops")))
#else
#define NO_PK
#endif
#define PIN(x) asm volatile("" : "+v"(x))
#define MFMA(a, b, c) __builtin_amdgcn_mfma_f32_16x16x32_bf16((a), (b), (c), 0, 0, 0)

struct K1Args {
  const char* x[2]; const char* k[2]; const char* v[2]; const char* wq[2]; const char* wo[2];
  char* ows;
};

#define SB() __builtin_amdgcn_sched_barrier(0)
__device__ __forceinline__ void dma16x3(unsigned lds, unsigned v0, unsigned v1, unsigned v2, const char* base) {
  asm volatile("s_mov_b32 m0, %0\n\ts_nop 0\n\tglobal_load_lds_dwordx4 %1, %4\n\t"
               "global_load_lds_dwordx4 %2, %4 offset:1024\n\tglobal_load_lds_dwordx4 %3, %4 offset:2048"
               :: "s"(lds), "v"(v0), "v"(v1), "v"(v2), "s"(base) : "memory");
}

#define KVNT 1
template <int OFF, bool NT>
__device__ __forceinline__ void dma16o(unsigned lds, unsigned v, const char* base) {
  if (NT) asm volatile("s_mov_b32 m0, %0\n\ts_nop 0\n\tglobal_load_lds_dwordx4 %1, %2 offset:%3 nt"
                       :: "s"(lds), "v"(v), "s"(base), "n"(OFF) : "memory");
  else asm volatile("s_mov_b32 m0, %0\n\ts_nop 0\n\tglobal_load_lds_dwordx4 %1, %2 offset:%3"
                    :: "s"(lds), "v"(v), "s"(base), "n"(OFF) : "memory");
}
template <bool NT>
__device__ __forceinline__ void dma16j(int j, unsigned lds, unsigned v, const char* base) {
  if (j == 0) dma16o<0, NT>(lds, v, base); else if (j == 1) dma16o<1024, NT>(lds, v, base); else dma16o<2048, NT>(lds, v, base);
}
__device__ __forceinline__ float max3f(float a, float b, float c) { return fmaxf(fmaxf(a, b), c); }

template <int G>
__device__ __forceinline__ void k1_body(const K1Args& A, int cg, int br, int b, int h) {
  const int lane = lane_id();
  STAMP_INIT();
  constexpr int NQ = G == 0 ? 5 : 4, QT0 = G == 0 ? 0 : 5;
  constexpr int NP = G == 0 ? 4 : 5, PT0 = G == 0 ? 0 : 4;
  constexpr int NM = 5;
  constexpr int MT0 = G == 0 ? 0 : 4;
  constexpr int QO = QT0 - MT0;
  const float cs = 0.07216878364870322f * 1.4426950408889634f;
  const int li = lane & 15, g = lane >> 4;
  const unsigned fv2 = (unsigned)((((li >> 1) & 1) | ((li >> 2) & 2)) << 1);
  const unsigned fk = (unsigned)(li >> 1);
  unsigned kofs0 = (unsigned)((16 * cg + li) * 384) + (((unsigned)g ^ fv2) << 4);
  unsigned pofs0 = L_PB + (unsigned)((16 * PT0 + li) * 128) + (((unsigned)g ^ fk) << 4);
  unsigned vofs[3];
  {
    const int q = (lane >> 2) & 3, p = lane & 3;
    const unsigned fv = (unsigned)(((q >> 1) & 1) | ((g & 1) << 1));
#pragma unroll
    for (int i = 0; i < 3; ++i) {
      unsigned t = (unsigned)(3 * cg + i);
      vofs[i] = (unsigned)((8 * g + q) * 384) + ((t ^ fv) << 5) + (unsigned)(p * 8);
    }
  }
  const unsigned pw = L_PB + (unsigned)((16 * QT0 + li) * 128) + ((((unsigned)(2 * cg + (g >> 1))) ^ fk) << 4) + (unsigned)((g & 1) * 8);
  const unsigned mofs = L_M + (unsigned)((16 * MT0 + li) * 4);
  unsigned dofs[6];
  unsigned kvo[3];
  unsigned xofs = 0;
  auto mk_dofs = [&]() {
    const int ln = lane_id();
#pragma unroll
    for (int j = 0; j < 6; ++j) {
      int s = 64 * j + ln, r = (s * 2731) >> 16, c = s - r * 24;
      int f = (((r >> 1) & 1) | ((r >> 2) & 2)) << 1;
      int ch = c ^ f;
      dofs[j] = (unsigned)((16 * cg + r) * (G == 0 ? NH * DH * 2 : 384) + ch * 16 - (j % 3) * 1024);
    }
    if (G == 0) xofs = (unsigned)((ln >> 3) * (DM * 2) + (((ln & 7) ^ ((4 * (cg & 1) + (ln >> 4)) & 7)) << 4));
  };
  auto mk_kvo = [&]() {
    const int ln = lane_id();
#pragma unroll
    for (int j = 0; j < 3; ++j) {
      int s = 64 * j + ln, r = (s * 2731) >> 16, c = s - r * 24;
      kvo[j] = (unsigned)((16 * cg + r) * 384 + (c ^ (((r >> 1) & 1) << 1)) * 16 - j * 1024);
    }
  };
  mk_dofs();

  const size_t kvoff = (size_t)(b * NH + h) * SK * DH * 2;
  constexpr size_t KV4 = (size_t)4 * SK * DH * 2;
  const char* xb = A.x[br] + (size_t)b * SQ * DM * 2;
  const char* wq0 = A.wq[br] + (size_t)h * DH * 2;
  const char* kb0 = A.k[br] + kvoff;
  const char* vb0 = A.v[br] + kvoff;
  auto issue_unit = [&](const char* src, int unit) {
    dma16x3((unsigned)(unit * UNIT + cg * 6144), dofs[0], dofs[1], dofs[2], src);
    dma16x3((unsigned)(unit * UNIT + cg * 6144 + 3072), dofs[3], dofs[4], dofs[5], src);
  };
  auto issue_kv = [&](const char* src, int unit) {
    if (G == 0) dma16x3((unsigned)(unit * UNIT + cg * 6144), kvo[0], kvo[1], kvo[2], src);
    else dma16x3((unsigned)(unit * UNIT + cg * 6144 + 3072), dofs[3], dofs[4], dofs[5], src);
  };
  auto unit1 = [&](const char* src, int unit, int j) {
    dma16j<G == 1 && KVNT>(j % 3, (unsigned)(unit * UNIT + cg * 6144 + (j / 3) * 3072), dofs[j], src);
  };
  auto kv1 = [&](const char* src, int unit, int j) {
    if (G == 0) dma16j<KVNT>(j, (unsigned)(unit * UNIT + cg * 6144), kvo[j], src);
    else dma16j<KVNT>(j, (unsigned)(unit * UNIT + cg * 6144 + 3072), dofs[3 + j], src);
  };
  auto x1 = [&](const char* src, int xo, int i) {
    int p = cg + 4 * i; if (p > 17) p -= 2;
    dma16((unsigned)(L_PB + xo + p * 1024), xofs, src + (size_t)p * 8 * DM * 2);
  };
  auto issue_x = [&](const char* src, int pb) {
#pragma unroll
    for (int i = 0; i < 5; ++i) {
      int p = cg + 4 * i; if (p > 17) p -= 2;
      dma16((unsigned)(L_PB + pb * PBUF + p * 1024), xofs, src + (size_t)p * 8 * DM * 2);
    }
  };

#define PRV 3
#define PRIO(v, p) do { if (PRV == (v)) __builtin_amdgcn_s_setprio(p); } while (0)
  if (G == 1) PRIO(0, 1);
  if (G == 0) {
    const char* wb = wq0;
    issue_unit(wb, 2);
    issue_x(xb, 0);
    issue_unit(wb + (size_t)64 * NH * DH * 2, 3);
    issue_unit(wb + (size_t)128 * NH * DH * 2, 4);
    WAIT_VM(12);
  } else {
    issue_unit(kb0, 1);
    if (cg == 0 && lane < 2) *(LDS3 int*)(uintptr_t)(L_F + lane * 4) = 0;
  }
  BAR();

  int cv = 0, cp = 0, ck = 0;
  auto set_v = [&](int unit) { const int o = unit * UNIT; bump(vofs[0], o - cv); bump(vofs[1], o - cv); bump(vofs[2], o - cv); cv = o; };
  auto set_p = [&](int o) { bump(pofs0, o - cp); cp = o; };
  auto set_k = [&](int unit) { const int o = unit * UNIT; bump(kofs0, o - ck); ck = o; };
  f32x4 acc[NP][3];
  bf16x8 fa[2][3], fb[2][NP], fr[4];
  auto qphase = [&](int km, int unit, int xo, bool mm, bool rd, auto&& slot) {
    if (km == 1 && rd) { set_v(unit); set_p(xo); }
    const unsigned pa = km ? pofs0 : x64(pofs0);
#pragma unroll
    for (int m = 0; m < 3 * NP; ++m) {
      if (mm) acc[m / 3][m % 3] = MFMA(fa[km][m % 3], fb[km][m / 3], acc[m / 3][m % 3]);
      SB();
      if (rd) {
        if (m < 3) {
          const unsigned va = vofs[m] + (1 - km) * 12288;
          fa[1 - km][m] = lds_tr2(va, va + 1536);
        } else if (m < 3 + NP) fb[1 - km][m - 3] = lds_b128(pa + (m - 3) * 2048);
      }
      slot(m);
      SB();
    }
  };
  auto noslot = [&](int) {};

  for (int it = 0; it < 2; ++it) {
    const char* wb = wq0 + it * 4 * DH * 2;
    const char* kb = kb0 + it * KV4;
    const char* vb = vb0 + it * KV4;
#pragma unroll
    for (int t = 0; t < NP; ++t)
#pragma unroll
      for (int i = 0; i < 3; ++i) acc[t][i] = zero_acc();
    {
      int tid = G * 256 + cg * 64 + lane_id();
      if (tid < 2 * SQ) *(LDS3 float*)(uintptr_t)(L_M + tid * 4) = -__builtin_inff();
    }
    qphase(1, 2, 0, false, true, [&](int m) { if (G == 0 && m >= 3 && m < 8) x1(xb + 128, PBUF, m - 3); });
    qphase(0, 2, 0, true, true, [&](int m) { if (G == 0 && (m & 1) && m < 10) x1(xb + 256, -L_PB, m >> 1); });
    if (G == 0) WAIT_VM(5);
    BAR();
    int wu = 3;
#pragma unroll 1
    for (int j = 1; j <= 7; ++j) {
      const char* wn = wb + (size_t)(j + 2) * 64 * NH * DH * 2;
      const int un = wu == 2 ? 4 : wu - 1;
      const int xo = wu == 4 ? -L_PB : (wu - 2) * PBUF, xn = un == 4 ? -L_PB : (un - 2) * PBUF;
      qphase(1, wu, xo, true, true, [&](int m) { if (G == 0 && m < 10 && (m & 1)) x1(xb + (j + 2) * 128, xn, m >> 1); if (G == 0 && m == 11) unit1(wn, un, 0); });
      qphase(0, wu, xo, true, true, [&](int m) { if (G == 0 && !(m & 1) && m < 10) unit1(wn, un, 1 + (m >> 1)); });
      if (G == 0) WAIT_VM(11);
      BAR();
      wu = wu == 4 ? 2 : wu + 1;
    }
    qphase(1, 4, -L_PB, true, true, noslot);
    qphase(0, 4, -L_PB, true, true, noslot);
    if (G == 0) WAIT_VM(0); else mk_dofs();
    BAR();
    qphase(1, 2, 0, true, true, [&](int m) { if (G == 1 && (m & 1) && m < 12) unit1(kb + UNIT, 0, m >> 1); });
    qphase(0, 2, 0, true, true, [&](int m) { if (G == 1 && (m & 1) && m < 12) unit1(kb + 2 * UNIT, 3, m >> 1); });
    BAR();
    if (G == 0) mk_kvo();
    qphase(1, 0, 0, true, false, [&](int m) { if (m % 3 == 1 && m < 9) kv1(vb, 2, m / 3); });
    const int lq = lane_id(), liq = lq & 15, gq = lq >> 4;
    const unsigned fkq = (unsigned)(liq >> 1);
#pragma unroll
    for (int t = 0; t < NP; ++t)
#pragma unroll
      for (int i = 0; i < 3; ++i) {
        unsigned ch = (unsigned)(2 * (3 * cg + i) + (gq >> 1));
        unsigned a = L_Q + (unsigned)((16 * (PT0 + t) + liq) * 384) + ((ch ^ fkq) << 4) + (unsigned)((gq & 1) * 8);
        *(LDS3 u32x2*)(uintptr_t)a = pack4(acc[t][i][0] * cs, acc[t][i][1] * cs, acc[t][i][2] * cs, acc[t][i][3] * cs);
        acc[t][i] = zero_acc();
      }
    BAR();
    bf16x8 qf[NQ][6];
    {
      unsigned q0 = L_Q + (unsigned)((16 * QT0 + liq) * 384) + (((unsigned)gq ^ fkq) << 4);
#pragma unroll
      for (int t = 0; t < NQ; ++t)
#pragma unroll
        for (int ks = 0; ks < 6; ++ks) qf[t][ks] = lds_b128(((ks & 1) ? (q0 ^ 64u) : q0) + t * 6144 + (ks >> 1) * 128);
    }
    if (G == 1) WAIT_VM(15);
    BAR();
    issue_kv(vb + UNIT, 4);

    constexpr float THR = 32.0f, ZR = 24.0f;
    bool nz = false;
    unsigned k1a = 0, p1a = 0;
    f32x4 s[NQ];
    const f32x4 zero4 = {0.f, 0.f, 0.f, 0.f};
    float mc[NM], lsum[NQ], alpha[NP];
    bf16x8 kf[3];
    int flg = 0;
    bool pend = false;
#pragma unroll
    for (int t = 0; t < NM; ++t) mc[t] = 0.f;
#pragma unroll
    for (int t = 0; t < NQ; ++t) lsum[t] = 0.f;
#pragma unroll
    for (int t = 0; t < NP; ++t) alpha[t] = 1.f;

    auto qk_pre = [&](int unit) {
      set_k(unit);
      k1a = x64(kofs0);
      kf[0] = lds_b128(kofs0);
      kf[1] = lds_b128(k1a);
    };
    auto qk = [&](int unit, auto&& slot) {
#pragma unroll
      for (int ks = 0; ks < 6; ++ks) {
#pragma unroll
        for (int t = 0; t < NQ; ++t) {
          s[t] = MFMA(kf[ks % 3], qf[t][ks], ks == 0 ? zero4 : s[t]);
          SB();
          if (t == 0 && ks + 2 < 6) kf[(ks + 2) % 3] = lds_b128(((ks & 1) ? k1a : kofs0) + ((ks + 2) >> 1) * 128);
          if (t >= 1) slot(ks * 4 + t);
          SB();
        }
      }
    };
    auto check = [&](int par, int gen, bool first) {
      if (nz) {
#pragma unroll
        for (int t = 0; t < NQ; ++t)
#pragma unroll
          for (int e = 0; e < 4; ++e) s[t][e] += mc[QO + t];
      }
      float mx = fmaxf(s[0][0], s[0][1]);
#pragma unroll
      for (int t = 0; t < NQ; ++t) mx = t ? max3f(max3f(mx, s[t][0], s[t][1]), s[t][2], s[t][3]) : max3f(mx, s[0][2], s[0][3]);
      if (first || __builtin_amdgcn_ballot_w64(!(mx <= THR)) != 0) {
        const unsigned mb = addv(mofs, par * SQ * 4);
#pragma unroll
        for (int t = 0; t < NQ; ++t) {
          float y = fmaxf(fmaxf(s[t][0], s[t][1]), fmaxf(s[t][2], s[t][3])) - mc[QO + t];
          __hip_atomic_fetch_max((LDS3 float*)(uintptr_t)(mb + (unsigned)((QO + t) * 64)), y,
                                 __ATOMIC_RELAXED, __HIP_MEMORY_SCOPE_WORKGROUP);
        }
        *(volatile LDS3 int*)(uintptr_t)(L_F + par * 4) = gen;
      }
    };
    auto ld_flag = [&](int par) { flg = *(volatile LDS3 int*)(uintptr_t)(L_F + par * 4); };
    auto update = [&](int par, int gen, bool first) {
      if (first || __builtin_amdgcn_readfirstlane(flg) == gen) {
        const unsigned mb = addv(mofs, par * SQ * 4);
#pragma unroll
        for (int t = 0; t < NM; ++t) {
          float m = *(const LDS3 float*)(uintptr_t)(mb + (unsigned)(t * 64));
          float mn = first ? (fabsf(m) <= ZR ? 0.f : -m) : fminf(mc[t], -m);
          float d = mn - mc[t];
          mc[t] = mn;
          float al = __builtin_amdgcn_exp2f(d);
          if (t >= QO && t < QO + NQ) {
#pragma unroll
            for (int e = 0; e < 4; ++e) s[t - QO][e] += d;
            if (!first) lsum[t - QO] *= al;
          }
          if (!first && t < NP) alpha[t] = al;
        }
        if (!first) { pend = true; nz = true; }
        else {
          bool z = true;
#pragma unroll
          for (int t = 0; t < NQ; ++t) z = z && mc[QO + t] == 0.f;
          nz = __builtin_amdgcn_ballot_w64(!z) != 0;
        }
      }
    };
    auto softmax = [&](int par, auto&& slot) {
      const unsigned pwb = addv(pw, par * PBUF);
#pragma unroll
      for (int t = 0; t < NQ; ++t) {
        float p0 = __builtin_amdgcn_exp2f(s[t][0]);
        float p1 = __builtin_amdgcn_exp2f(s[t][1]);
        SB();
        slot(t);
        SB();
        float p2 = __builtin_amdgcn_exp2f(s[t][2]);
        float p3 = __builtin_amdgcn_exp2f(s[t][3]);
        lsum[t] += (p0 + p1) + (p2 + p3);
        PIN(lsum[t]);
        *(LDS3 u32x2*)(uintptr_t)(pwb + (unsigned)(t * 2048)) = pack4(p0, p1, p2, p3);
        SB();
      }
    };
    auto rescale = [&]() {
      if (pend) {
        asm volatile("s_nop 7\n\ts_nop 7\n\ts_nop 3" ::: "memory");
#pragma unroll
        for (int t = 0; t < NP; ++t)
#pragma unroll
          for (int i = 0; i < 3; ++i) {
#pragma unroll
            for (int e = 0; e < 4; ++e) {
              float v = acc[t][i][e];
              asm volatile("v_mul_f32 %0, %0, %1" : "+v"(v) : "v"(alpha[t]));
              acc[t][i][e] = v;
            }
          }
        asm volatile("s_nop 1" ::: "memory");
        pend = false;
      }
    };
    auto ld_v = [&](int ks, int unit, int i) {
      if (ks == 0 && i == 0) set_v(unit);
      const unsigned va = vofs[i] + ks * 12288;
      fa[ks][i] = lds_tr2(va, va + 1536);
    };
    auto rs = [&](int st, int sh) { return (st >= NP ? st + sh : st) & 3; };
    constexpr int SH = G == 0 ? 0 : 2;
    auto ld_p = [&](int par, int st, int sh = G == 0 ? 0 : 2) {
      if (st == 0) set_p(par * PBUF);
      if (st == NP) p1a = x64(pofs0);
      fr[rs(st, sh)] = lds_b128((st >= NP ? p1a : pofs0) + (st % NP) * 2048);
    };
    auto pv_steps = [&](int s0, int s1, int sh, auto&& slot) {
#pragma unroll
      for (int st = s0; st < s1; ++st)
#pragma unroll
        for (int i = 0; i < 3; ++i) {
          acc[st % NP][i] = MFMA(fa[st / NP][i], fr[rs(st, sh)], acc[st % NP][i]);
          SB();
          slot(st * 3 + i);
          SB();
        }
    };
    const int gen0 = it * 8 + 1;

    qk_pre(1);
    qk(1, noslot);
    check(0, gen0, true);
    if (G == 1) WAIT_VM(12);
    BAR();
    qk_pre(0);
    update(0, gen0, true);
    SB();
    softmax(0, noslot);
    qk(0, noslot);
    check(1, gen0 + 1, false);
    WAIT_VM(3);
    BAR();
    int uv = 2;
    {
#pragma unroll
      for (int i = 0; i < 3; ++i) fa[1][i] = __builtin_bit_cast(bf16x8, zero_acc());
#pragma unroll
      for (int i = 0; i < 4; ++i) fr[i] = __builtin_bit_cast(bf16x8, zero_acc());
    }
    const char* wnx = wq0 + 4 * DH * 2;
    auto interval = [&](int c, auto kind_c) {
      constexpr int KIND = decltype(kind_c)::value;
      constexpr bool LAST = KIND == 3;
      const int uk = uv == 4 ? 0 : uv + 1;
      const int u3 = uv >= 2 ? uv - 2 : uv + 3;
      const int u4 = uv >= 1 ? uv - 1 : uv + 4;
      const int pp = (c - 1) & 1;
      const char* src3 = KIND <= 1 ? kb + (size_t)(c + 2) * UNIT : vb + (size_t)7 * UNIT;
      const char* src4 = vb + (size_t)(c + 1) * UNIT;
      auto d3 = [&](int j) { if (KIND == 1 || KIND == 2) kv1(src3, u3, j); };
      auto d4 = [&](int j) { if (KIND <= 1) kv1(src4, u4, j); };
      if (G == 0) {
        ld_flag(c & 1);
        pv_steps(6, 8, SH, [&](int m) {
          if (m == 18) ld_v(0, uv, 0);
          if (m == 19) ld_p(pp, 0);
          if (m == 20) ld_v(0, uv, 1);
          if (m == 21) ld_v(0, uv, 2);
          if (m == 22) ld_p(pp, 1);
        });
        if (LAST) mk_dofs();
        rescale();
        update(c & 1, gen0 + c, false);
        SB();
        PRIO(2, 1); PRIO(3, 1);
        softmax(c & 1, [&](int t) { if (LAST) { if (it == 0) unit1(wnx, 2, t); } else if (t < 3) d3(t); });
        PRIO(2, 0); PRIO(3, 0);
        pv_steps(0, LAST ? 6 : 5, SH, [&](int m) {
          const int st = m / 3, i = m % 3;
          if (i == 0 && st + 2 < (LAST ? 8 : 6)) ld_p(pp, st + 2);
          if (i == 1 && st < 3) ld_v(1, uv, st);
          if (i == 2 && st < 3) d4(st);
          if (!LAST && m == 10) qk_pre(uk);
          if (LAST && it == 0) {
            if (m == 2) unit1(wnx, 2, 5);
            if (m >= 5 && m < 17 && (m & 1)) unit1(wnx + (size_t)64 * NH * DH * 2, 3, (m - 5) >> 1);
          }
        });
        if (!LAST) {
          qk(uk, [&](int m) { if (m == 18) ld_p(pp, 6); if (m == 22) ld_p(pp, 7); });
          pv_steps(5, 6, SH, noslot);
          check((c + 1) & 1, gen0 + c + 1, false);
          WAIT_VM(3);
        }
      } else {
        PRIO(1, 1);
        ld_flag(c & 1);
        pv_steps(7, 10, SH, [&](int m) {
          if (m == 21) ld_v(0, uv, 0);
          if (m == 22) ld_p(pp, 0);
          if (m == 23) ld_v(0, uv, 1);
          if (m == 24) ld_v(0, uv, 2);
          if (m == 25) ld_p(pp, 1);
          if (m == 26) d3(0);
          if (m == 27) ld_p(pp, 2);
          if (m == 28) d3(1);
        });
        rescale();
        pv_steps(0, 5, SH, [&](int m) {
          if (m == 0) ld_p(pp, 3);
          if (m == 1) d3(2);
          if (m == 3) ld_p(pp, 4);
          if (!LAST && m == 10) qk_pre(uk);
        });
        update(c & 1, gen0 + c, false);
        SB();
        PRIO(2, 1); PRIO(3, 1);
        softmax(c & 1, [&](int t) { if (t < 3) d4(t); });
        PRIO(1, 0); PRIO(2, 0);
        if (KIND == 2 && it == 0) { mk_dofs(); issue_unit(kb0 + KV4, u4); }
        if (KIND == 2 && it == 1) {
          const int ln = lane_id();
          const char* wsrc = A.wo[br] + (size_t)(h >> 3) * 1536 * DM * 2 + (size_t)(((b * 4 + (h & 3)) * 4 + cg) * 120) * 128;
          asm volatile("s_mov_b32 m0, %0\n\ts_nop 0\n\tglobal_load_lds_dword %1, %3\n\tglobal_load_lds_dword %2, %3"
                       :: "s"(L_PF), "v"((unsigned)ln * 128u), "v"((unsigned)(ln < 56 ? 64 + ln : 119) * 128u), "s"(wsrc) : "memory");
        }
        if (!LAST) {
          qk(uk, [&](int m) {
            if (m == 13) ld_v(1, uv, 0);
            if (m == 14) ld_p(pp, 5);
            if (m == 17) ld_v(1, uv, 1);
            if (m == 18) ld_v(1, uv, 2);
            if (m == 21) ld_p(pp, 6);
            if (m == 22) ld_p(pp, 7);
          });
          pv_steps(5, 6, SH, [&](int m) { if (m == 15) ld_p(pp, 8); });
          check((c + 1) & 1, gen0 + c + 1, false);
        } else {
          ld_v(1, uv, 0); ld_p(pp, 5); ld_v(1, uv, 1); ld_v(1, uv, 2); ld_p(pp, 6); ld_p(pp, 7);
          pv_steps(5, 6, SH, [&](int m) { if (m == 15) ld_p(pp, 8); });
        }
        SB();
        PRIO(3, 0);
        pv_steps(6, 7, SH, [&](int m) { if (m == 18) ld_p(pp, 9); });
        if (!LAST) { if (KIND == 2) { if (it == 0) WAIT_VM(9); else WAIT_VM(5); } else WAIT_VM(3); }
      }
      uv = uv >= 3 ? uv - 3 : uv + 2;
    };
#pragma unroll 1
    for (int c = 1; c <= 5; ++c) {
      interval(c, std::integral_constant<int, 1>{});
      BAR();
    }
    interval(6, std::integral_constant<int, 2>{});
    BAR();
    interval(7, std::integral_constant<int, 3>{});
    const int le = lane_id();
#pragma unroll
    for (int t = 0; t < NQ; ++t) {
      float l = lsum[t];
      l += __builtin_bit_cast(float, __builtin_amdgcn_ds_bpermute((le ^ 16) << 2, __builtin_bit_cast(int, l)));
      l += __builtin_bit_cast(float, __builtin_amdgcn_ds_bpermute((le ^ 32) << 2, __builtin_bit_cast(int, l)));
      if (le < 16) *(LDS3 float*)(uintptr_t)(L_L + (unsigned)(cg * SQ * 4 + (16 * (QT0 + t) + le) * 4)) = l;
    }
    if (G == 0) { if (it == 0) WAIT_VM(12); else WAIT_VM(0); }
    else { if (it == 0) WAIT_VM(6); else WAIT_VM(0); }
    BAR();
    if (G == 0) {
      if (it == 0) issue_x(xb, 0);
      pv_steps(6, 8, SH, [&](int m) {
        if (m == 18) ld_v(0, 0, 0);
        if (m == 19) ld_p(1, 0);
        if (m == 20) ld_v(0, 0, 1);
        if (m == 21) ld_v(0, 0, 2);
        if (m == 22) ld_p(1, 1);
      });
      rescale();
      pv_steps(0, 8, SH, [&](int m) {
        const int st = m / 3, i = m % 3;
        if (i == 0 && st + 2 < 8) ld_p(1, st + 2);
        if (i == 1 && st < 3) ld_v(1, 0, st);
        if (it == 0 && i == 2 && st < 6) unit1(wnx + (size_t)128 * NH * DH * 2, 4, st);
      });
    } else {
      pv_steps(7, 10, SH, [&](int m) {
        if (m == 21) ld_v(0, 0, 0);
        if (m == 22) ld_p(1, 0);
        if (m == 23) ld_v(0, 0, 1);
        if (m == 24) ld_v(0, 0, 2);
        if (m == 25) ld_p(1, 1);
        if (m == 27) ld_p(1, 2);
      });
      rescale();
      pv_steps(0, 10, 0, [&](int m) {
        const int st = m / 3, i = m % 3;
        if (i == 0 && st + 3 < 10) ld_p(1, st + 3, 0);
        if (i == 1 && st < 3) ld_v(1, 0, st);
      });
    }
    {
      const __amdgpu_buffer_rsrc_t ors =
          __builtin_amdgcn_make_buffer_rsrc(A.ows + (size_t)((br * NB + b) * NH + h + 4 * it) * OWS_ITEM, 0, (int)OWS_ITEM, 0x00020000);
      const int ufull = (3 * cg + 1) >> 1;
      const int uhalf = (cg & 1) ? (3 * cg) >> 1 : (3 * cg + 2) >> 1;
#pragma unroll
      for (int t = 0; t < NP; ++t) {
        unsigned la = L_L + (unsigned)((16 * (PT0 + t) + (le & 15)) * 4);
        float l = (*(const LDS3 float*)(uintptr_t)la + *(const LDS3 float*)(uintptr_t)(la + SQ * 4)) +
                  (*(const LDS3 float*)(uintptr_t)(la + 2 * SQ * 4) + *(const LDS3 float*)(uintptr_t)(la + 3 * SQ * 4));
        float inv = 1.0f / l;
        u32x2 o[3];
#pragma unroll
        for (int i = 0; i < 3; ++i) o[i] = pack4(acc[t][i][0] * inv, acc[t][i][1] * inv, acc[t][i][2] * inv, acc[t][i][3] * inv);
        u32x4 full; u32x2 half;
        if (cg & 1) { full = (u32x4){o[1][0], o[1][1], o[2][0], o[2][1]}; half = o[0]; }
        else        { full = (u32x4){o[0][0], o[0][1], o[1][0], o[1][1]}; half = o[2]; }
#define OWT 0
        __builtin_amdgcn_raw_buffer_store_b128(full, ors, le * 16 + t * 2048, (ufull >> 1) * PBUF + PT0 * 2048 + (ufull & 1) * 1024, OWT);
        __builtin_amdgcn_raw_buffer_store_b64(half, ors, le * 16 + t * 2048, (uhalf >> 1) * PBUF + PT0 * 2048 + (uhalf & 1) * 1024 + (cg & 1) * 8, OWT);
      }
    }
    if (G == 0 && it == 0) WAIT_VM(14);
    BAR();
  }
}

NO_PK __global__ __launch_bounds__(512, 2) void k1_attn(K1Args A) {
  extern __shared__ __attribute__((aligned(16))) char shm[];
  const int w = __builtin_amdgcn_readfirstlane(threadIdx.x >> 6);
  const int xcd = blockIdx.x & 7, idx = blockIdx.x >> 3;
  const int br = xcd >> 2, h = (xcd & 3) * 8 + (idx & 3), b = idx >> 2;
  if (w < 4) k1_body<0>(A, w, br, b, h);
  else k1_body<1>(A, w - 4, br, b, h);
}

constexpr int K2_STAGE = PBUF + 64 * 320;
constexpr int K2_NST = 4;
constexpr int K2_CH = 24;
struct K2Args { const char* wo[2]; const char* ows; float* part; };

template <int NR, int NC, bool WARM>
__device__ __forceinline__ void k2_body(const K2Args& A, int w, int lane, int rt0, int ct0, int br, int b, int nb, int sp) {
  STAMP_INIT();
  const int g = lane >> 4;
  unsigned dofs[6]; int dlds[6]; bool isw[6];
#pragma unroll
  for (int i = 0; i < 6; ++i) {
    int p = (w < 4 ? w : w - 1) + 7 * i; if (p >= 38) p -= 4;
    isw[i] = p >= 18;
    dlds[i] = p * 1024;
    if (p < 18) dofs[i] = (unsigned)(p * 1024 + lane * 16);
    else {
      int s = 64 * (p - 18) + lane, r = s / 20, c = s - r * 20;
      dofs[i] = (unsigned)(r * (DM * 2) + ((c ^ (((r >> 2) & 1) << 1)) << 4));
    }
  }
  const char* ob = A.ows + ((size_t)(br * NB + b) * NH * 3 + (size_t)sp * K2_CH) * PBUF;
  const char* wb = A.wo[br] + (size_t)sp * K2_CH * 64 * DM * 2 + nb * 320;
  auto issue1 = [&](int ci, int i) {
    if (!WARM) dma16((unsigned)((ci & 3) * K2_STAGE) + (unsigned)dlds[i], dofs[i], isw[i] ? wb + (size_t)ci * 64 * DM * 2 : ob + (size_t)ci * PBUF);
  };
  auto issue = [&](int ci) {
#pragma unroll
    for (int i = 0; i < 6; ++i) issue1(ci, i);
  };
#define WD 2
  const int wl = lane < 36 ? lane : 35, wr = lane < 24 ? lane : 23;
  const unsigned wo1 = (unsigned)(nb * 4608 + wl * 128), wo2 = (unsigned)((8 * b + wr / 3) * (DM * 2) + (wr % 3) * 128);
  auto warm = [&](int c) {
    if (WARM && c < K2_CH)
      asm volatile("s_mov_b32 m0, %0\n\ts_nop 0\n\tglobal_load_lds_dword %1, %2\n\tglobal_load_lds_dword %3, %4"
                   :: "s"(K2_NST * K2_STAGE), "v"(wo1), "s"(ob + (size_t)c * PBUF), "v"(wo2), "s"(wb + (size_t)c * 64 * DM * 2) : "memory");
  };
  unsigned aofs[NC];
  {
    const int q = (lane >> 2) & 3, p = lane & 3;
#pragma unroll
    for (int i = 0; i < NC; ++i) aofs[i] = PBUF + (unsigned)((4 * g + q) * 320 + (((ct0 + i) ^ (g & 1)) << 5) + p * 8);
  }
  const unsigned bofs = (unsigned)(rt0 * 2048 + lane * 16);
  f32x4 acc[NR][NC];
#pragma unroll
  for (int t = 0; t < NR; ++t)
#pragma unroll
    for (int i = 0; i < NC; ++i) acc[t][i] = (f32x4){0.f, 0.f, 0.f, 0.f};
  bf16x8 fa[2][NC], fb[2][NR];
  auto ld_one = [&](int ks, unsigned base, int r) {
    if (r < NC) fa[ks][r] = lds_tr2(base + aofs[r] + ks * 10240, base + aofs[r] + ks * 10240 + 5120);
    else if (r < NC + NR) fb[ks][r - NC] = lds_b128(base + bofs + (r - NC) * 2048 + ks * 1024);
  };
  auto phase = [&](int km, unsigned base, bool rd, int cd, int d0, int nd) {
#pragma unroll
    for (int m = 0; m < NR * NC; ++m) {
      acc[m / NC][m % NC] = MFMA(fa[km][m % NC], fb[km][m / NC], acc[m / NC][m % NC]);
      SB();
      if (rd) ld_one(1 - km, base, m);
      constexpr int GAP = NR * NC >= 12 ? 4 : 3;
      if (cd >= 0 && m % GAP == 1 && m / GAP < nd) issue1(cd, d0 + m / GAP);
      SB();
    }
  };
  issue(0); issue(1); issue(2);
#pragma unroll
  for (int i = 3; i < 3 + WD; ++i) warm(i);
  if (!WARM) WAIT_VM(12);
  BAR();
#pragma unroll
  for (int r = 0; r < NC + NR; ++r) ld_one(0, 0u, r);
  SB();
  issue(3);
  SB();
  phase(0, 0u, true, -1, 0, 0);
  warm(3 + WD);
  if (!WARM) WAIT_VM(12);
  BAR();
  for (int ci = 1; ci < K2_CH - 3; ++ci) {
    unsigned base = (unsigned)((ci & 3) * K2_STAGE);
    phase(1, base, true, ci + 3, 0, 3);
    phase(0, base, true, ci + 3, 3, 3);
    warm(ci + 3 + WD);
    if (!WARM) WAIT_VM(12);
    BAR();
  }
#pragma unroll
  for (int ci = K2_CH - 3; ci < K2_CH; ++ci) {
    unsigned base = (unsigned)((ci & 3) * K2_STAGE);
    phase(1, base, true, -1, 0, 0);
    phase(0, base, true, -1, 0, 0);
    if (ci == K2_CH - 3) { if (!WARM) WAIT_VM(6); BAR(); } else if (ci == K2_CH - 2) { WAIT_VM(0); BAR(); }
  }
  phase(1, 0u, false, -1, 0, 0);
  float* pb = A.part + ((size_t)(((br * NB + b) * 4 + nb) * 4 + sp) * 90) * 256 + lane * 4;
#pragma unroll
  for (int t = 0; t < NR; ++t)
#pragma unroll
    for (int i = 0; i < NC; ++i) *(f32x4*)(pb + ((rt0 + t) * 10 + ct0 + i) * 256) = acc[t][i];
}

__global__ __launch_bounds__(512, 2) void k2_oproj(K2Args A) {
  extern __shared__ __attribute__((aligned(16))) char shm[];
  const int w = __builtin_amdgcn_readfirstlane(threadIdx.x >> 6);
  const int lane = threadIdx.x & 63;
  const int xcd = blockIdx.x & 7, idx = blockIdx.x >> 3;
  const int br = xcd >> 2, sp = xcd & 3, b = idx >> 2, nb = idx & 3;
  const int cg = w & 3;
  if (w < 2) k2_body<5, 3, false>(A, w, lane, 0, 3 * cg, br, b, nb, sp);
  else if (w < 4) k2_body<5, 2, false>(A, w, lane, 0, 2 * cg + 2, br, b, nb, sp);
  else if (w == 4) k2_body<4, 2, true>(A, w, lane, 5, 2 * cg, br, b, nb, sp);
  else if (w == 5) k2_body<4, 2, false>(A, w, lane, 5, 2 * cg, br, b, nb, sp);
  else k2_body<4, 3, false>(A, w, lane, 5, 3 * cg - 2, br, b, nb, sp);
}

__global__ __launch_bounds__(256) void k3_reduce(const float* __restrict__ part, char* __restrict__ out) {
  int gid = blockIdx.x * 256 + threadIdx.x;
  int lane = gid & 63, f = (gid >> 6) % 90, tile = (gid >> 6) / 90;
  const float* p = part + ((size_t)tile * 4 * 90 + f) * 256 + lane * 4;
  f32x4 s = *(const f32x4*)p;
  s += *(const f32x4*)(p + 90 * 256);
  s += *(const f32x4*)(p + 2 * 90 * 256);
  s += *(const f32x4*)(p + 3 * 90 * 256);
  int nb = tile & 3, b = (tile >> 2) & 7, br = tile >> 5;
  int rt = f / 10, ct = f - rt * 10;
  int q = rt * 16 + (lane & 15), n = br * DM + nb * 160 + ct * 16 + (lane >> 4) * 4;
  *(u32x2*)(out + ((size_t)(b * SQ + q) * (2 * DM) + n) * 2) = pack4(s[0], s[1], s[2], s[3]);
}

extern "C" void kernel_launch(void* const* d_in, const int* in_sizes, int n_in, void* d_out, int out_size,
                              void* d_ws, size_t ws_size, hipStream_t stream) {
  (void)in_sizes; (void)n_in; (void)out_size; (void)ws_size;
  K1Args a1;
  for (int i = 0; i < 2; ++i) {
    a1.x[i] = (const char*)d_in[5 * i + 0];
    a1.k[i] = (const char*)d_in[5 * i + 1];
    a1.v[i] = (const char*)d_in[5 * i + 2];
    a1.wq[i] = (const char*)d_in[5 * i + 3];
    a1.wo[i] = (const char*)d_in[5 * i + 4];
  }
  a1.ows = (char*)d_ws;
  K2Args a2;
  a2.wo[0] = (const char*)d_in[4];
  a2.wo[1] = (const char*)d_in[9];
  a2.ows = (const char*)d_ws;
  a2.part = (float*)((char*)d_ws + OWS_BYTES);
  (void)hipFuncSetAttribute((const void*)k1_attn, hipFuncAttributeMaxDynamicSharedMemorySize, L_TOT1);
  (void)hipFuncSetAttribute((const void*)k2_oproj, hipFuncAttributeMaxDynamicSharedMemorySize, K2_NST * K2_STAGE + 256);
  k1_attn<<<256, 512, L_TOT1, stream>>>(a1);
  k2_oproj<<<256, 512, K2_NST * K2_STAGE + 256, stream>>>(a2);
  k3_reduce<<<64 * 90 * 64 / 256, 256, 0, stream>>>(a2.part, (char*)d_out);
}
```
